```python
import torch

def _softmax(x, axis=-1):
    return torch.softmax(x, dim=axis)

def _gelu(x):
    return torch.nn.functional.gelu(x, approximate='tanh')

def _silu(x):
    return torch.nn.functional.silu(x)

def _sigmoid(x):
    return torch.sigmoid(x)

def _layernorm(x, axis=-1, eps=1e-05):
    m = x.mean(dim=axis, keepdim=True)
    v = x.var(dim=axis, keepdim=True, unbiased=False)
    return (x - m) / torch.sqrt(v + eps)

def _scatter_add(tgt, idx, src, axis=0):
    return tgt.clone().index_add_(axis, idx, src)

def _segment_sum(src, seg, n):
    out = torch.zeros((n,) + src.shape[1:], dtype=src.dtype)
    return out.index_add_(0, seg, src)

def _pad(x, pw):
    flat = tuple((v for pair in reversed(pw) for v in pair))
    return torch.nn.functional.pad(x, flat)
kernel_name = 'recurrent_scan__memory_aug_recurrent_mem_tr__mixer_channel_mlp__prompt'

def setup_inputs():

    _w = lambda a: (lambda t: t.to(torch.bfloat16) if t.is_floating_point() else t)(torch.as_tensor(a))

    def _A_setup():
        B, S, Dm = (1, 2048, 512)
        H, Dh = (32, 128)
        M, seg = (8, 32)
        seg = min(seg, S)
        S = S // seg * seg
        return {'x': _w((lambda *s: torch.randn(*s, dtype=torch.bfloat16) * 0.05)(B, S, Dm)), 'mem0': _w((lambda *s: torch.randn(*s, dtype=torch.bfloat16) * 0.8)(B, M, Dm)), 'Wq': _w((lambda *s: torch.randn(*s, dtype=torch.bfloat16) * 0.8)(Dm, H, Dh) * Dm ** (-0.5)), 'Wk': _w((lambda *s: torch.randn(*s, dtype=torch.bfloat16) * 0.8)(Dm, H, Dh) * Dm ** (-0.5)), 'Wv': _w((lambda *s: torch.randn(*s, dtype=torch.bfloat16) * 0.8)(Dm, H, Dh) * Dm ** (-0.5)), 'Wo': _w((lambda *s: torch.randn(*s, dtype=torch.bfloat16) * 0.8)(H, Dh, Dm) * (H * Dh) ** (-0.5)), 'seg_len': seg}
    _A_in = _A_setup()

    def _B_setup():
        B, S, D, F = (1, 2048, 512, 1376)
        return {'x': _w((lambda *s: torch.randn(*s, dtype=torch.bfloat16) * 0.05)(B, S, D)), 'w1': _w((lambda *s: torch.randn(*s, dtype=torch.bfloat16) * 0.8)(D, F) * D ** (-0.5)), 'b1': _w((lambda *s: torch.randn(*s, dtype=torch.bfloat16) * 0.4)(F)), 'w2': _w((lambda *s: torch.randn(*s, dtype=torch.bfloat16) * 0.2)(F, D) * F ** (-0.5)), 'b2': _w((lambda *s: torch.randn(*s, dtype=torch.bfloat16) * 0.2)(D))}
    _B_in = _B_setup()
    return {'A_x': _A_in['x'], 'A_mem0': _A_in['mem0'], 'A_Wq': _A_in['Wq'], 'A_Wk': _A_in['Wk'], 'A_Wv': _A_in['Wv'], 'A_Wo': _A_in['Wo'], 'A_seg_len': _A_in['seg_len'], 'B_x': _B_in['x'], 'B_w1': _B_in['w1'], 'B_b1': _B_in['b1'], 'B_w2': _B_in['w2'], 'B_b2': _B_in['b2']}

def reference(A_x, A_mem0, A_Wq, A_Wk, A_Wv, A_Wo, A_seg_len, B_x, B_w1, B_b1, B_w2, B_b2):

    def _A_ref(x, mem0, Wq, Wk, Wv, Wo, seg_len):
        B, S, Dm = x.shape
        H, Dh = (Wq.shape[1], Wq.shape[2])
        M = mem0.shape[1]
        scale = Dh ** (-0.5)
        n_seg = S // seg_len
        L = M + seg_len
        idx = torch.arange(L)
        mask = (idx[:, None] < M) | (idx[:, None] >= idx[None, :])
        mem = mem0
        outs = []
        for s in range(n_seg):
            xs = x[:, s * seg_len:(s + 1) * seg_len, :].clone()
            h = torch.concatenate([mem, xs], axis=1)
            q = torch.einsum('btd,dhe->bhte', h, Wq)
            k = torch.einsum('btd,dhe->bhte', h, Wk)
            v = torch.einsum('btd,dhe->bhte', h, Wv)
            scores = torch.einsum('bhqe,bhke->bhqk', q, k) * scale
            scores = torch.where(mask[None, None], scores, -1000000000.0)
            p = _softmax(scores, axis=-1)
            o = torch.einsum('bhqk,bhke->bhqe', p, v)
            o = torch.einsum('bhte,hed->btd', o, Wo)
            mem = o[:, :M, :].clone()
            outs.append(o[:, M:, :])
        return torch.concatenate(outs, axis=1)

    def _B_ref(x, w1, b1, w2, b2):
        y = _layernorm(x, axis=-1)
        h = _gelu(torch.einsum('bsd,df->bsf', y, w1) + b1)
        return torch.einsum('bsf,fd->bsd', h, w2) + b2
    x = A_x
    xn = _layernorm(x, axis=-1)
    s = _A_ref(xn, A_mem0, A_Wq, A_Wk, A_Wv, A_Wo, A_seg_len)
    g = _silu(_B_ref(xn, B_w1, B_b1, B_w2, B_b2))
    return x + s * g
```

```cpp
#include <hip/hip_runtime.h>
#include <hip/hip_bf16.h>
#include <stdint.h>

typedef __attribute__((ext_vector_type(8))) __bf16 bf16x8;
typedef __attribute__((ext_vector_type(4))) __bf16 bf16x4;
typedef __attribute__((ext_vector_type(4))) float f32x4;
typedef __attribute__((ext_vector_type(4))) short s16x4;
typedef __attribute__((ext_vector_type(4))) unsigned u32x4;
typedef __attribute__((ext_vector_type(2))) unsigned u32x2;
typedef unsigned short u16;
#define LAS __attribute__((address_space(3)))

static constexpr int S_ = 2048, DM = 512, NH = 32, DH = 128, MM = 8, SEG = 32, NSEG = 64, FF = 1376;
static constexpr int NQKV = 3 * NH * DH;

__device__ __forceinline__ float bf2f(u16 v) { return __uint_as_float(((unsigned)v) << 16); }
__device__ __forceinline__ u16 f2bf(float f) {
  __bf16 b = (__bf16)f;
  return __builtin_bit_cast(u16, b);
}
__device__ __forceinline__ float rbf(float f) { return bf2f(f2bf(f)); }
typedef __attribute__((ext_vector_type(2))) float f32x2;
typedef __attribute__((ext_vector_type(2))) __bf16 bf16x2;
__device__ __forceinline__ unsigned pack2(float a, float b) {
  bf16x2 r = __builtin_convertvector((f32x2){a, b}, bf16x2);
  return __builtin_bit_cast(unsigned, r);
}

__global__ __launch_bounds__(256) void k_prep(const u16* __restrict__ x, u16* __restrict__ xn, u16* __restrict__ y,
                                              float* __restrict__ sums, unsigned* __restrict__ cnt) {
  int tid = threadIdx.x, w = tid >> 6, l = tid & 63;
  int row = blockIdx.x * 4 + w;
  {
    int gt = blockIdx.x * 256 + tid;
    for (int i = gt; i < (NSEG + 1) * MM * DM; i += 512 * 256) sums[i] = 0.f;
    if (gt < 8192) cnt[gt] = 0u;
  }
  u32x4 raw = *(const u32x4*)(x + (size_t)row * DM + l * 8);
  float v[8];
#pragma unroll
  for (int i = 0; i < 4; ++i) { v[2 * i] = bf2f(raw[i] & 0xffff); v[2 * i + 1] = bf2f(raw[i] >> 16); }
#pragma unroll
  for (int pass = 0; pass < 2; ++pass) {
    float s = 0.f;
#pragma unroll
    for (int i = 0; i < 8; ++i) s += v[i];
#pragma unroll
    for (int m = 32; m >= 1; m >>= 1) s += __shfl_xor(s, m);
    float mean = s * (1.f / DM);
    float q = 0.f;
#pragma unroll
    for (int i = 0; i < 8; ++i) { float d = v[i] - mean; q += d * d; }
#pragma unroll
    for (int m = 32; m >= 1; m >>= 1) q += __shfl_xor(q, m);
    float var = rbf(q * (1.f / DM));
    float mb = rbf(mean);
    float den = rbf(sqrtf(rbf(var + 1e-5f)));
#pragma unroll
    for (int i = 0; i < 8; ++i) v[i] = rbf(rbf(v[i] - mb) / den);
    u32x4 o;
#pragma unroll
    for (int i = 0; i < 4; ++i) o[i] = pack2(v[2 * i], v[2 * i + 1]);
    *(u32x4*)((pass == 0 ? xn : y) + (size_t)row * DM + l * 8) = o;
  }
}

struct EpiStore {
  u16* C; int ldc; int pad_;
  __device__ __forceinline__ void operator()(int m, int n, f32x4 a) const {
    u32x2 o; o[0] = pack2(a[0], a[1]); o[1] = pack2(a[2], a[3]);
    *(u32x2*)(C + (size_t)m * ldc + n) = o;
  }
};
struct EpiGelu {
  u16* C; int ldc; int pad_; const u16* b;
  __device__ __forceinline__ void operator()(int m, int n, f32x4 a) const {
    float r[4];
#pragma unroll
    for (int i = 0; i < 4; ++i) {
      float v = rbf(rbf(a[i]) + bf2f(b[n + i]));
      float u = 0.7978845608028654f * (v + 0.044715f * v * v * v);
      r[i] = 0.5f * v * (1.f + tanhf(u));
    }
    u32x2 o; o[0] = pack2(r[0], r[1]); o[1] = pack2(r[2], r[3]);
    *(u32x2*)(C + (size_t)m * ldc + n) = o;
  }
};
struct EpiSilu {
  u16* C; int ldc; int pad_; const u16* b;
  __device__ __forceinline__ void operator()(int m, int n, f32x4 a) const {
    float r[4];
#pragma unroll
    for (int i = 0; i < 4; ++i) {
      float v = rbf(rbf(a[i]) + bf2f(b[n + i]));
      r[i] = v / (1.f + __expf(-v));
    }
    u32x2 o; o[0] = pack2(r[0], r[1]); o[1] = pack2(r[2], r[3]);
    *(u32x2*)(C + (size_t)m * ldc + n) = o;
  }
};
struct EpiFinal {
  u16* C; int ldc; int pad_; const u16* g; const u16* x;
  __device__ __forceinline__ void operator()(int m, int n, f32x4 a) const {
    u32x2 gg = *(const u32x2*)(g + (size_t)m * ldc + n), xx = *(const u32x2*)(x + (size_t)m * ldc + n);
    float gf[4] = {bf2f(gg[0] & 0xffff), bf2f(gg[0] >> 16), bf2f(gg[1] & 0xffff), bf2f(gg[1] >> 16)};
    float xf[4] = {bf2f(xx[0] & 0xffff), bf2f(xx[0] >> 16), bf2f(xx[1] & 0xffff), bf2f(xx[1] >> 16)};
    float r[4];
#pragma unroll
    for (int i = 0; i < 4; ++i) r[i] = xf[i] + rbf(rbf(a[i]) * gf[i]);
    u32x2 o; o[0] = pack2(r[0], r[1]); o[1] = pack2(r[2], r[3]);
    *(u32x2*)(C + (size_t)m * ldc + n) = o;
  }
};

template <int BM, int BN, int BK>
struct GemmCfg {
  static constexpr int ASTR = BK * 2 + 16;
  static constexpr int BSTR = BN * 2 + 32;
  static constexpr int ABYTES = BM * ASTR, BBYTES = BK * BSTR;
  static constexpr int LDS = 2 * (ABYTES + BBYTES);
};

template <int BM, int BN, int BK, class Epi>
__device__ __forceinline__ void gemm_tile(char* smem, const u16* __restrict__ A, int lda, const u16* __restrict__ B, int ldb,
                                          int M, int N, int K, int bm, int bn, const Epi& epi) {
  using C = GemmCfg<BM, BN, BK>;
  constexpr int WM = BM / 2, WN = BN / 2, TM = WM / 16, TN = WN / 16;
  constexpr int ACH = BM * BK / 8 / 256;
  constexpr int BCH = BK * BN / 8 / 256;
  static_assert(ACH >= 1 && BCH >= 1, "tile too small");
  const int tid = threadIdx.x, w = tid >> 6, l = tid & 63, g = l >> 4, c16 = l & 15;
  const int wm = w >> 1, wn = w & 1;
  f32x4 acc[TM][TN];
#pragma unroll
  for (int i = 0; i < TM; ++i)
#pragma unroll
    for (int j = 0; j < TN; ++j) acc[i][j] = (f32x4){0, 0, 0, 0};
  u32x4 ra[ACH], rb[BCH];
  auto gload = [&](int k0) {
#pragma unroll
    for (int i = 0; i < ACH; ++i) {
      int ch = tid + i * 256, r = ch / (BK / 8), c = ch % (BK / 8);
      int k = k0 + c * 8;
      ra[i] = (k < K) ? *(const u32x4*)(A + (size_t)(bm + r) * lda + k) : (u32x4){0, 0, 0, 0};
    }
#pragma unroll
    for (int i = 0; i < BCH; ++i) {
      int ch = tid + i * 256, r = ch / (BN / 8), c = ch % (BN / 8);
      int k = k0 + r, n = bn + c * 8;
      rb[i] = (k < K && n < N) ? *(const u32x4*)(B + (size_t)k * ldb + n) : (u32x4){0, 0, 0, 0};
    }
  };
  auto lstore = [&](int buf) {
    char* sa = smem + buf * (C::ABYTES + C::BBYTES);
    char* sb = sa + C::ABYTES;
#pragma unroll
    for (int i = 0; i < ACH; ++i) {
      int ch = tid + i * 256, r = ch / (BK / 8), c = ch % (BK / 8);
      *(u32x4*)(sa + r * C::ASTR + c * 16) = ra[i];
    }
#pragma unroll
    for (int i = 0; i < BCH; ++i) {
      int ch = tid + i * 256, r = ch / (BN / 8), c = ch % (BN / 8);
      *(u32x4*)(sb + r * C::BSTR + ((c ^ (((r >> 3) & 1) * (BN / 16))) * 16)) = rb[i];
    }
  };
  const int nk = (K + BK - 1) / BK;
  gload(0);
  lstore(0);
  __syncthreads();
  for (int kt = 0; kt < nk; ++kt) {
    int buf = kt & 1;
    if (kt + 1 < nk) gload((kt + 1) * BK);
    const char* sa = smem + buf * (C::ABYTES + C::BBYTES);
    const char* sb = sa + C::ABYTES;
#pragma unroll
    for (int ks = 0; ks < BK / 32; ++ks) {
      bf16x8 af[TM], bfr[TN];
#pragma unroll
      for (int i = 0; i < TM; ++i)
        af[i] = *(const bf16x8*)(sa + (wm * WM + i * 16 + c16) * C::ASTR + (ks * 32 + g * 8) * 2);
#pragma unroll
      for (int j = 0; j < TN; ++j) {
        int q = c16 >> 2, p = c16 & 3;
        int col = wn * WN + j * 16 + p * 4;
        s16x4 lo, hi;
        {
          int r = ks * 32 + g * 8 + q;
          lo = __builtin_amdgcn_ds_read_tr16_b64_v4i16((LAS s16x4*)(sb + r * C::BSTR + ((col * 2) ^ (((r >> 3) & 1) * BN))));
          r += 4;
          hi = __builtin_amdgcn_ds_read_tr16_b64_v4i16((LAS s16x4*)(sb + r * C::BSTR + ((col * 2) ^ (((r >> 3) & 1) * BN))));
        }
        typedef __attribute__((ext_vector_type(8))) short s16x8;
        s16x8 t = __builtin_shufflevector(lo, hi, 0, 1, 2, 3, 4, 5, 6, 7);
        bfr[j] = __builtin_bit_cast(bf16x8, t);
      }
#pragma unroll
      for (int i = 0; i < TM; ++i)
#pragma unroll
        for (int j = 0; j < TN; ++j) acc[i][j] = __builtin_amdgcn_mfma_f32_16x16x32_bf16(bfr[j], af[i], acc[i][j], 0, 0, 0);
    }
    if (kt + 1 < nk) lstore(buf ^ 1);
    __syncthreads();
  }
#pragma unroll
  for (int i = 0; i < TM; ++i)
#pragma unroll
    for (int j = 0; j < TN; ++j) {
      int m = bm + wm * WM + i * 16 + c16, n = bn + wn * WN + j * 16 + g * 4;
      if (n < N) epi(m, n, acc[i][j]);
    }
}

template <int BM, int BN, int BK, class Epi>
__global__ __launch_bounds__(256) void k_gemm(const u16* __restrict__ A, int lda, const u16* __restrict__ B, int ldb, int M,
                                              int N, int K, Epi epi) {
  extern __shared__ __attribute__((aligned(16))) char smem[];
  gemm_tile<BM, BN, BK, Epi>(smem, A, lda, B, ldb, M, N, K, blockIdx.y * BM, blockIdx.x * BN, epi);
}

template <int BM, int BN, int BK>
__global__ __launch_bounds__(256) void k_gemm_qkv(const u16* __restrict__ A, const u16* __restrict__ Wq,
                                                  const u16* __restrict__ Wk, const u16* __restrict__ Wv, u16* __restrict__ C) {
  extern __shared__ __attribute__((aligned(16))) char smem[];
  int bn = blockIdx.x * BN;
  int which = bn / 4096;
  const u16* B = which == 0 ? Wq : (which == 1 ? Wk : Wv);
  EpiStore epi{C + which * 4096, NQKV, 0};
  gemm_tile<BM, BN, BK, EpiStore>(smem, A, DM, B, 4096, S_, 4096, DM, blockIdx.y * BM, bn - which * 4096, epi);
}

__device__ __forceinline__ unsigned swz(unsigned row, unsigned ch) {
  return 256u * row + 16u * (ch ^ (((row & 3) << 2) | ((row >> 2) & 3)));
}

static constexpr int L_WO = 0;
static constexpr int L_V = 131072;
static constexpr int L_K = L_V + 40 * 256;
static constexpr int L_Q = L_K + 40 * 256;
static constexpr int L_MEM = L_Q + 8 * 256;
static constexpr int L_END = L_MEM + 8704;
static_assert(L_END <= 163840, "lds");

__global__ __launch_bounds__(256, 1) void k_recur(const u16* __restrict__ Wq, const u16* __restrict__ Wk,
                                                  const u16* __restrict__ Wv, const u16* __restrict__ Wo,
                                                  const u16* __restrict__ mem0, const u16* __restrict__ qkv,
                                                  u16* __restrict__ kvm, float* sums, unsigned* cnt) {
  extern __shared__ __attribute__((aligned(16))) char smem[];
  const int h = blockIdx.x, tid = threadIdx.x, w = tid >> 6, l = tid & 63, g = l >> 4, c16 = l & 15;
  const int q4 = c16 >> 2, p4 = c16 & 3;
  typedef __attribute__((ext_vector_type(8))) short s16x8;

  bf16x8 wf[3][2][16];
#pragma unroll
  for (int m = 0; m < 3; ++m) {
    const u16* W = m == 0 ? Wq : (m == 1 ? Wk : Wv);
    for (int i = 0; i < 32; ++i) {
      int ch = tid + i * 256, r = ch >> 4, c = ch & 15;
      u32x4 v = *(const u32x4*)(W + (size_t)r * 4096 + h * 128 + c * 8);
      *(u32x4*)(smem + swz(r, c)) = v;
    }
    __syncthreads();
#pragma unroll
    for (int c = 0; c < 2; ++c)
#pragma unroll
      for (int t = 0; t < 16; ++t) {
        int ct = 2 * w + c;
        int r0 = 32 * t + 8 * g + q4;
        s16x4 lo = __builtin_amdgcn_ds_read_tr16_b64_v4i16((LAS s16x4*)(smem + swz(r0, 2 * ct + (p4 >> 1)) + 8 * (p4 & 1)));
        s16x4 hi = __builtin_amdgcn_ds_read_tr16_b64_v4i16((LAS s16x4*)(smem + swz(r0 + 4, 2 * ct + (p4 >> 1)) + 8 * (p4 & 1)));
        s16x8 tt = __builtin_shufflevector(lo, hi, 0, 1, 2, 3, 4, 5, 6, 7);
        wf[m][c][t] = __builtin_bit_cast(bf16x8, tt);
      }
    __syncthreads();
  }
  for (int t = 0; t < 4; ++t) {
    char* st = smem + 131072;
    for (int i = 0; i < 8; ++i) {
      int ch = tid + i * 256, r = ch >> 6, c = ch & 63;
      u32x4 v = *(const u32x4*)(Wo + (size_t)(h * 128 + t * 32 + r) * 512 + c * 8);
      *(u32x4*)(st + (c >> 4) * 8192 + swz(r, c & 15)) = v;
    }
    __syncthreads();
#pragma unroll
    for (int j = 0; j < 8; ++j) {
      int r0 = 8 * g + q4;
      s16x4 lo = __builtin_amdgcn_ds_read_tr16_b64_v4i16((LAS s16x4*)(st + w * 8192 + swz(r0, 2 * j + (p4 >> 1)) + 8 * (p4 & 1)));
      s16x4 hi = __builtin_amdgcn_ds_read_tr16_b64_v4i16((LAS s16x4*)(st + w * 8192 + swz(r0 + 4, 2 * j + (p4 >> 1)) + 8 * (p4 & 1)));
      s16x8 tt = __builtin_shufflevector(lo, hi, 0, 1, 2, 3, 4, 5, 6, 7);
      *(s16x8*)(smem + L_WO + ((w * 8 + j) * 4 + t) * 1024 + l * 16) = tt;
    }
    __syncthreads();
  }

  const __amdgpu_buffer_rsrc_t srs = __builtin_amdgcn_make_buffer_rsrc(sums, 0, (NSEG + 1) * MM * DM * 4, 0x00020000);
  const float scale = 0.08838834764831845f;

  u32x4 pk[2], pv[2];
  auto kvload = [&](int s) {
#pragma unroll
    for (int i = 0; i < 2; ++i) {
      int ch = tid + i * 256, r = ch >> 4, c = ch & 15;
      const u16* src = qkv + (size_t)(s * SEG + r) * NQKV + 4096 + h * 128 + c * 8;
      pk[i] = *(const u32x4*)src;
      pv[i] = *(const u32x4*)(src + 4096);
    }
  };
  auto kvstore = [&]() {
#pragma unroll
    for (int i = 0; i < 2; ++i) {
      int ch = tid + i * 256, r = ch >> 4, c = ch & 15;
      *(u32x4*)(smem + L_K + swz(8 + r, c)) = pk[i];
      *(u32x4*)(smem + L_V + swz(8 + r, c)) = pv[i];
    }
  };
  kvload(0);
  kvstore();

  for (int s = 0; s < NSEG; ++s) {
    int tid = threadIdx.x;
    asm volatile("" : "+v"(tid));
    const int w = tid >> 6, l = tid & 63, g = l >> 4, c16 = l & 15;
    const int q4 = c16 >> 2, p4 = c16 & 3;
    auto kvload = [&](int s) {
#pragma unroll
      for (int i = 0; i < 2; ++i) {
        int ch = tid + i * 256, r = ch >> 4, c = ch & 15;
        const u16* src = qkv + (size_t)(s * SEG + r) * NQKV + 4096 + h * 128 + c * 8;
        pk[i] = *(const u32x4*)src;
        pv[i] = *(const u32x4*)(src + 4096);
      }
    };
    auto kvstore = [&]() {
#pragma unroll
      for (int i = 0; i < 2; ++i) {
        int ch = tid + i * 256, r = ch >> 4, c = ch & 15;
        *(u32x4*)(smem + L_K + swz(8 + r, c)) = pk[i];
        *(u32x4*)(smem + L_V + swz(8 + r, c)) = pv[i];
      }
    };
    if (s == 0) {
#pragma unroll
      for (int i = 0; i < 2; ++i) {
        int ch = tid + i * 256, r = ch >> 6, c = ch & 63;
        u32x4 v = *(const u32x4*)(mem0 + r * 512 + c * 8);
        *(u32x4*)(smem + L_MEM + r * 1088 + c * 16) = v;
      }
    } else {
      if (tid == 0) {
        unsigned spins = 0;
        while (__hip_atomic_load(&cnt[s * 64], __ATOMIC_RELAXED, __HIP_MEMORY_SCOPE_AGENT) < (unsigned)NH) {
          __builtin_amdgcn_s_sleep(1);
          if (++spins > (1u << 24)) break;
        }
      }
      __syncthreads();
#pragma unroll
      for (int i = 0; i < 2; ++i) {
        int ch = tid + i * 256, r = ch >> 6, c = ch & 63;
        unsigned off = (unsigned)((s * MM + r) * DM + c * 8) * 4;
        u32x4 a = __builtin_amdgcn_raw_buffer_load_b128(srs, off, 0, 16);
        u32x4 b = __builtin_amdgcn_raw_buffer_load_b128(srs, off + 16, 0, 16);
        u32x4 v;
        v[0] = pack2(__uint_as_float(a[0]), __uint_as_float(a[1]));
        v[1] = pack2(__uint_as_float(a[2]), __uint_as_float(a[3]));
        v[2] = pack2(__uint_as_float(b[0]), __uint_as_float(b[1]));
        v[3] = pack2(__uint_as_float(b[2]), __uint_as_float(b[3]));
        *(u32x4*)(smem + L_MEM + r * 1088 + c * 16) = v;
      }
    }
    __syncthreads();
    f32x4 acc[3][2];
#pragma unroll
    for (int m = 0; m < 3; ++m)
#pragma unroll
      for (int c = 0; c < 2; ++c) acc[m][c] = (f32x4){0, 0, 0, 0};
#pragma unroll
    for (int t = 0; t < 16; ++t) {
      int r = c16 & 7;
      bf16x8 mf = *(const bf16x8*)(smem + L_MEM + r * 1088 + (4 * t + g) * 16);
#pragma unroll
      for (int m = 0; m < 3; ++m)
#pragma unroll
        for (int c = 0; c < 2; ++c) acc[m][c] = __builtin_amdgcn_mfma_f32_16x16x32_bf16(wf[m][c][t], mf, acc[m][c], 0, 0, 0);
    }
    if (s + 1 < NSEG) kvload(s + 1);
    if (c16 < 8) {
#pragma unroll
      for (int c = 0; c < 2; ++c) {
        int col = 32 * w + 16 * c + 4 * g;
        int chunk = col >> 3, sub = (col & 7) * 2;
        u32x2 vq, vk, vv;
        vq[0] = pack2(acc[0][c][0], acc[0][c][1]); vq[1] = pack2(acc[0][c][2], acc[0][c][3]);
        vk[0] = pack2(acc[1][c][0], acc[1][c][1]); vk[1] = pack2(acc[1][c][2], acc[1][c][3]);
        vv[0] = pack2(acc[2][c][0], acc[2][c][1]); vv[1] = pack2(acc[2][c][2], acc[2][c][3]);
        *(u32x2*)(smem + L_Q + swz(c16, chunk) + sub) = vq;
        *(u32x2*)(smem + L_K + swz(c16, chunk) + sub) = vk;
        *(u32x2*)(smem + L_V + swz(c16, chunk) + sub) = vv;
        u16* dst = kvm + (size_t)(s * MM + c16) * 8192 + h * 128 + col;
        *(u32x2*)dst = vk;
        *(u32x2*)(dst + 4096) = vv;
      }
    }
    __syncthreads();
    f32x4 sc[3];
#pragma unroll
    for (int kt = 0; kt < 3; ++kt) sc[kt] = (f32x4){0, 0, 0, 0};
#pragma unroll
    for (int t = 0; t < 4; ++t) {
      bf16x8 qf = *(const bf16x8*)(smem + L_Q + swz(c16 & 7, 4 * t + g));
#pragma unroll
      for (int kt = 0; kt < 3; ++kt) {
        bf16x8 kf = *(const bf16x8*)(smem + L_K + swz(16 * kt + c16, 4 * t + g));
        sc[kt] = __builtin_amdgcn_mfma_f32_16x16x32_bf16(kf, qf, sc[kt], 0, 0, 0);
      }
    }
    float mx = -3.0e38f;
#pragma unroll
    for (int kt = 0; kt < 3; ++kt)
#pragma unroll
      for (int r = 0; r < 4; ++r) {
        float v = rbf(rbf(sc[kt][r]) * scale);
        if (kt == 2 && g >= 2) v = -3.0e38f;
        sc[kt][r] = v;
        mx = fmaxf(mx, v);
      }
    mx = fmaxf(mx, __shfl_xor(mx, 16));
    mx = fmaxf(mx, __shfl_xor(mx, 32));
    float sum = 0.f;
#pragma unroll
    for (int kt = 0; kt < 3; ++kt)
#pragma unroll
      for (int r = 0; r < 4; ++r) {
        float e = (kt == 2 && g >= 2) ? 0.f : __expf(sc[kt][r] - mx);
        sc[kt][r] = e;
        sum += e;
      }
    sum += __shfl_xor(sum, 16);
    sum += __shfl_xor(sum, 32);
    float inv = 1.f / sum;
    u32x4 pb0, pb1;
    pb0[0] = pack2(sc[0][0] * inv, sc[0][1] * inv); pb0[1] = pack2(sc[0][2] * inv, sc[0][3] * inv);
    pb0[2] = pack2(sc[1][0] * inv, sc[1][1] * inv); pb0[3] = pack2(sc[1][2] * inv, sc[1][3] * inv);
    pb1[0] = pack2(sc[2][0] * inv, sc[2][1] * inv); pb1[1] = pack2(sc[2][2] * inv, sc[2][3] * inv);
    pb1[2] = 0; pb1[3] = 0;
    bf16x8 pf0 = __builtin_bit_cast(bf16x8, pb0), pf1 = __builtin_bit_cast(bf16x8, pb1);
    f32x4 oa[2];
#pragma unroll
    for (int c = 0; c < 2; ++c) {
      int ch = 2 * (2 * w + c) + (p4 >> 1);
      s16x4 a0 = __builtin_amdgcn_ds_read_tr16_b64_v4i16((LAS s16x4*)(smem + L_V + swz(4 * g + q4, ch) + 8 * (p4 & 1)));
      s16x4 a1 = __builtin_amdgcn_ds_read_tr16_b64_v4i16((LAS s16x4*)(smem + L_V + swz(16 + 4 * g + q4, ch) + 8 * (p4 & 1)));
      s16x4 a2 = __builtin_amdgcn_ds_read_tr16_b64_v4i16((LAS s16x4*)(smem + L_V + swz(32 + 4 * g + q4, ch) + 8 * (p4 & 1)));
      s16x8 v0 = __builtin_shufflevector(a0, a1, 0, 1, 2, 3, 4, 5, 6, 7);
      s16x8 v1 = __builtin_shufflevector(a2, a2, 0, 1, 2, 3, 4, 5, 6, 7);
      f32x4 o = (f32x4){0, 0, 0, 0};
      o = __builtin_amdgcn_mfma_f32_16x16x32_bf16(__builtin_bit_cast(bf16x8, v0), pf0, o, 0, 0, 0);
      o = __builtin_amdgcn_mfma_f32_16x16x32_bf16(__builtin_bit_cast(bf16x8, v1), pf1, o, 0, 0, 0);
      oa[c] = o;
    }
    if (c16 < 8) {
#pragma unroll
      for (int c = 0; c < 2; ++c) {
        int col = 32 * w + 16 * c + 4 * g;
        u32x2 v; v[0] = pack2(oa[c][0], oa[c][1]); v[1] = pack2(oa[c][2], oa[c][3]);
        *(u32x2*)(smem + L_MEM + swz(c16, col >> 3) + (col & 7) * 2) = v;
      }
    }
    __syncthreads();
    if (s + 1 < NSEG) {
      bf16x8 of[4];
#pragma unroll
      for (int t = 0; t < 4; ++t) of[t] = *(const bf16x8*)(smem + L_MEM + swz(c16 & 7, 4 * t + g));
      float* dst = sums + (size_t)(s + 1) * MM * DM;
#pragma unroll
      for (int j = 0; j < 8; ++j) {
        f32x4 d = (f32x4){0, 0, 0, 0};
#pragma unroll
        for (int t = 0; t < 4; ++t) {
          bf16x8 bfw = *(const bf16x8*)(smem + L_WO + ((w * 8 + j) * 4 + t) * 1024 + l * 16);
          d = __builtin_amdgcn_mfma_f32_16x16x32_bf16(of[t], bfw, d, 0, 0, 0);
        }
        if (g < 2) {
#pragma unroll
          for (int r = 0; r < 4; ++r)
            __hip_atomic_fetch_add(dst + (4 * g + r) * DM + 128 * w + 16 * j + c16, d[r], __ATOMIC_RELAXED, __HIP_MEMORY_SCOPE_AGENT);
        }
      }
      asm volatile("s_waitcnt vmcnt(0)" ::: "memory");
      kvstore();
      __syncthreads();
      if (tid == 0) __hip_atomic_fetch_add(&cnt[(s + 1) * 64], 1u, __ATOMIC_RELAXED, __HIP_MEMORY_SCOPE_AGENT);
    }
  }
}

__global__ __launch_bounds__(256) void k_attn(const u16* __restrict__ qkv, const u16* __restrict__ kvm, u16* __restrict__ ox) {
  __shared__ __attribute__((aligned(16))) char smem[4 * 48 * 256];
  const int tid = threadIdx.x, w = tid >> 6, l = tid & 63, g = l >> 4, c16 = l & 15;
  const int q4 = c16 >> 2, p4 = c16 & 3;
  typedef __attribute__((ext_vector_type(8))) short s16x8;
  const int task = blockIdx.x * 4 + w, s = task >> 5, h = task & 31;
  char* sv = smem + w * 48 * 256;
  for (int i = 0; i < 12; ++i) {
    int ch = l + i * 64, r = ch >> 4, c = ch & 15;
    u32x4 v = (u32x4){0, 0, 0, 0};
    if (r < 8) v = *(const u32x4*)(kvm + (size_t)(s * MM + r) * 8192 + 4096 + h * 128 + c * 8);
    else if (r < 40) v = *(const u32x4*)(qkv + (size_t)(s * SEG + r - 8) * NQKV + 8192 + h * 128 + c * 8);
    *(u32x4*)(sv + swz(r, c)) = v;
  }
  f32x4 sc[3][2];
#pragma unroll
  for (int kt = 0; kt < 3; ++kt)
#pragma unroll
    for (int qt = 0; qt < 2; ++qt) sc[kt][qt] = (f32x4){0, 0, 0, 0};
#pragma unroll
  for (int t = 0; t < 4; ++t) {
    bf16x8 qf[2], kf[3];
#pragma unroll
    for (int qt = 0; qt < 2; ++qt)
      qf[qt] = *(const bf16x8*)(qkv + (size_t)(s * SEG + 16 * qt + c16) * NQKV + h * 128 + 32 * t + 8 * g);
#pragma unroll
    for (int kt = 0; kt < 3; ++kt) {
      int key = 16 * kt + c16;
      const u16* src = key < 8 ? kvm + (size_t)(s * MM + key) * 8192 + h * 128
                               : qkv + (size_t)(s * SEG + (key < 40 ? key - 8 : 0)) * NQKV + 4096 + h * 128;
      kf[kt] = *(const bf16x8*)(src + 32 * t + 8 * g);
    }
#pragma unroll
    for (int kt = 0; kt < 3; ++kt)
#pragma unroll
      for (int qt = 0; qt < 2; ++qt) sc[kt][qt] = __builtin_amdgcn_mfma_f32_16x16x32_bf16(kf[kt], qf[qt], sc[kt][qt], 0, 0, 0);
  }
  const float scale = 0.08838834764831845f;
  bf16x8 pf0[2], pf1[2];
#pragma unroll
  for (int qt = 0; qt < 2; ++qt) {
    int qi = 8 + 16 * qt + c16;
    float mx = -3.0e38f;
#pragma unroll
    for (int kt = 0; kt < 3; ++kt)
#pragma unroll
      for (int r = 0; r < 4; ++r) {
        int key = 16 * kt + 4 * g + r;
        float v = rbf(rbf(sc[kt][qt][r]) * scale);
        if (key > qi) v = -1e9f;
        if (key >= 40) v = -3.0e38f;
        sc[kt][qt][r] = v;
        mx = fmaxf(mx, v);
      }
    mx = fmaxf(mx, __shfl_xor(mx, 16));
    mx = fmaxf(mx, __shfl_xor(mx, 32));
    float sum = 0.f;
#pragma unroll
    for (int kt = 0; kt < 3; ++kt)
#pragma unroll
      for (int r = 0; r < 4; ++r) {
        int key = 16 * kt + 4 * g + r;
        float e = (key >= 40) ? 0.f : __expf(sc[kt][qt][r] - mx);
        sc[kt][qt][r] = e;
        sum += e;
      }
    sum += __shfl_xor(sum, 16);
    sum += __shfl_xor(sum, 32);
    float inv = 1.f / sum;
    u32x4 b0, b1;
    b0[0] = pack2(sc[0][qt][0] * inv, sc[0][qt][1] * inv); b0[1] = pack2(sc[0][qt][2] * inv, sc[0][qt][3] * inv);
    b0[2] = pack2(sc[1][qt][0] * inv, sc[1][qt][1] * inv); b0[3] = pack2(sc[1][qt][2] * inv, sc[1][qt][3] * inv);
    b1[0] = pack2(sc[2][qt][0] * inv, sc[2][qt][1] * inv); b1[1] = pack2(sc[2][qt][2] * inv, sc[2][qt][3] * inv);
    b1[2] = 0; b1[3] = 0;
    pf0[qt] = __builtin_bit_cast(bf16x8, b0);
    pf1[qt] = __builtin_bit_cast(bf16x8, b1);
  }
  __builtin_amdgcn_s_waitcnt(0);
  __builtin_amdgcn_wave_barrier();
#pragma unroll
  for (int c = 0; c < 8; ++c) {
    int ch = 2 * c + (p4 >> 1);
    s16x4 a0 = __builtin_amdgcn_ds_read_tr16_b64_v4i16((LAS s16x4*)(sv + swz(4 * g + q4, ch) + 8 * (p4 & 1)));
    s16x4 a1 = __builtin_amdgcn_ds_read_tr16_b64_v4i16((LAS s16x4*)(sv + swz(16 + 4 * g + q4, ch) + 8 * (p4 & 1)));
    s16x4 a2 = __builtin_amdgcn_ds_read_tr16_b64_v4i16((LAS s16x4*)(sv + swz(32 + 4 * g + q4, ch) + 8 * (p4 & 1)));
    s16x8 v0 = __builtin_shufflevector(a0, a1, 0, 1, 2, 3, 4, 5, 6, 7);
    s16x8 v1 = __builtin_shufflevector(a2, a2, 0, 1, 2, 3, 4, 5, 6, 7);
#pragma unroll
    for (int qt = 0; qt < 2; ++qt) {
      f32x4 o = (f32x4){0, 0, 0, 0};
      o = __builtin_amdgcn_mfma_f32_16x16x32_bf16(__builtin_bit_cast(bf16x8, v0), pf0[qt], o, 0, 0, 0);
      o = __builtin_amdgcn_mfma_f32_16x16x32_bf16(__builtin_bit_cast(bf16x8, v1), pf1[qt], o, 0, 0, 0);
      u32x2 v; v[0] = pack2(o[0], o[1]); v[1] = pack2(o[2], o[3]);
      *(u32x2*)(ox + (size_t)(s * SEG + 16 * qt + c16) * 4096 + h * 128 + 16 * c + 4 * g) = v;
    }
  }
}

extern "C" void kernel_launch(void* const* d_in, const int* in_sizes, int n_in, void* d_out, int out_size, void* d_ws,
                              size_t ws_size, hipStream_t stream) {
  const u16* x = (const u16*)d_in[0];
  const u16* mem0 = (const u16*)d_in[1];
  const u16* Wq = (const u16*)d_in[2];
  const u16* Wk = (const u16*)d_in[3];
  const u16* Wv = (const u16*)d_in[4];
  const u16* Wo = (const u16*)d_in[5];
  const u16* w1 = (const u16*)d_in[8];
  const u16* b1 = (const u16*)d_in[9];
  const u16* w2 = (const u16*)d_in[10];
  const u16* b2 = (const u16*)d_in[11];
  u16* out = (u16*)d_out;
  char* ws = (char*)d_ws;
  size_t o = 0;
  auto take = [&](size_t bytes) { char* p = ws + o; o += (bytes + 255) & ~(size_t)255; return p; };
  unsigned* cnt = (unsigned*)take(8192 * 4);
  float* sums = (float*)take((size_t)(NSEG + 1) * MM * DM * 4);
  u16* xn = (u16*)take((size_t)S_ * DM * 2);
  u16* y = (u16*)take((size_t)S_ * DM * 2);
  u16* qkv = (u16*)take((size_t)S_ * NQKV * 2);
  u16* h1 = (u16*)take((size_t)S_ * FF * 2);
  u16* gbuf = (u16*)take((size_t)S_ * DM * 2);
  u16* kvm = (u16*)take((size_t)NSEG * MM * 8192 * 2);
  u16* ox = (u16*)take((size_t)S_ * 4096 * 2);

  k_prep<<<512, 256, 0, stream>>>(x, xn, y, sums, cnt);
  {
    using C = GemmCfg<128, 128, 64>;
    hipFuncSetAttribute((const void*)k_gemm_qkv<128, 128, 64>, hipFuncAttributeMaxDynamicSharedMemorySize, C::LDS);
    k_gemm_qkv<128, 128, 64><<<dim3(NQKV / 128, S_ / 128), 256, C::LDS, stream>>>(xn, Wq, Wk, Wv, qkv);
  }
  {
    using C = GemmCfg<64, 64, 32>;
    k_gemm<64, 64, 32, EpiGelu><<<dim3((FF + 63) / 64, S_ / 64), 256, C::LDS, stream>>>(y, DM, w1, FF, S_, FF, DM, EpiGelu{h1, FF, 0, b1});
    k_gemm<64, 64, 32, EpiSilu><<<dim3(DM / 64, S_ / 64), 256, C::LDS, stream>>>(h1, FF, w2, DM, S_, DM, FF, EpiSilu{gbuf, DM, 0, b2});
  }
  hipFuncSetAttribute((const void*)k_recur, hipFuncAttributeMaxDynamicSharedMemorySize, 163840);
  k_recur<<<NH, 256, 163840, stream>>>(Wq, Wk, Wv, Wo, mem0, qkv, kvm, sums, cnt);
  k_attn<<<NSEG * NH / 4, 256, 0, stream>>>(qkv, kvm, ox);
  {
    using C = GemmCfg<64, 64, 64>;
    k_gemm<64, 64, 64, EpiFinal><<<dim3(DM / 64, S_ / 64), 256, C::LDS, stream>>>(ox, 4096, Wo, DM, S_, DM, 4096, EpiFinal{out, DM, 0, gbuf, x});
  }
}
```

```cpp
#include <hip/hip_runtime.h>
#include <hip/hip_bf16.h>
#include <stdint.h>

typedef __attribute__((ext_vector_type(8))) __bf16 bf16x8;
typedef __attribute__((ext_vector_type(4))) __bf16 bf16x4;
typedef __attribute__((ext_vector_type(4))) float f32x4;
typedef __attribute__((ext_vector_type(4))) short s16x4;
typedef __attribute__((ext_vector_type(4))) unsigned u32x4;
typedef __attribute__((ext_vector_type(2))) unsigned u32x2;
typedef unsigned short u16;
#define LAS __attribute__((address_space(3)))

static constexpr int S_ = 2048, DM = 512, NH = 32, DH = 128, MM = 8, SEG = 32, NSEG = 64, FF = 1376;
static constexpr int NQKV = 3 * NH * DH;

__device__ __forceinline__ float bf2f(u16 v) { return __uint_as_float(((unsigned)v) << 16); }
__device__ __forceinline__ u16 f2bf(float f) {
  __bf16 b = (__bf16)f;
  return __builtin_bit_cast(u16, b);
}
__device__ __forceinline__ float rbf(float f) { return bf2f(f2bf(f)); }
typedef __attribute__((ext_vector_type(2))) float f32x2;
typedef __attribute__((ext_vector_type(2))) __bf16 bf16x2;
__device__ __forceinline__ unsigned pack2(float a, float b) {
  bf16x2 r = __builtin_convertvector((f32x2){a, b}, bf16x2);
  return __builtin_bit_cast(unsigned, r);
}

__global__ __launch_bounds__(256) void k_prep(const u16* __restrict__ x, u16* __restrict__ xn, u16* __restrict__ y,
                                              float* __restrict__ zb, int zn4) {
  int tid = threadIdx.x, w = tid >> 6, l = tid & 63;
  int row = blockIdx.x * 4 + w;
  {
    int gt = blockIdx.x * 256 + tid;
    for (int i = gt; i < zn4; i += 512 * 256) ((f32x4*)zb)[i] = (f32x4){0, 0, 0, 0};
  }
  u32x4 raw = *(const u32x4*)(x + (size_t)row * DM + l * 8);
  float v[8];
#pragma unroll
  for (int i = 0; i < 4; ++i) { v[2 * i] = bf2f(raw[i] & 0xffff); v[2 * i + 1] = bf2f(raw[i] >> 16); }
#pragma unroll
  for (int pass = 0; pass < 2; ++pass) {
    float s = 0.f;
#pragma unroll
    for (int i = 0; i < 8; ++i) s += v[i];
#pragma unroll
    for (int m = 32; m >= 1; m >>= 1) s += __shfl_xor(s, m);
    float mean = s * (1.f / DM);
    float q = 0.f;
#pragma unroll
    for (int i = 0; i < 8; ++i) { float d = v[i] - mean; q += d * d; }
#pragma unroll
    for (int m = 32; m >= 1; m >>= 1) q += __shfl_xor(q, m);
    float var = rbf(q * (1.f / DM));
    float mb = rbf(mean);
    float den = rbf(sqrtf(rbf(var + 1e-5f)));
#pragma unroll
    for (int i = 0; i < 8; ++i) v[i] = rbf(rbf(v[i] - mb) / den);
    u32x4 o;
#pragma unroll
    for (int i = 0; i < 4; ++i) o[i] = pack2(v[2 * i], v[2 * i + 1]);
    *(u32x4*)((pass == 0 ? xn : y) + (size_t)row * DM + l * 8) = o;
  }
}

struct Params {
  const u16 *x, *mem0, *Wq, *Wk, *Wv, *Wo, *w1, *b1, *w2, *b2;
  u16 *out, *xn, *y, *qkv, *h1, *g, *kvm, *ox;
  float *P1, *P2;
  unsigned* fl;
  unsigned long long* stamps;
};
#define RLX_AGENT __ATOMIC_RELAXED, __HIP_MEMORY_SCOPE_AGENT
static constexpr int F_TILE = 0;
static constexpr int F_QKV = 1024;
static constexpr int F_H = 1280;
static constexpr int F_G = 1536;
static constexpr int F_M = 2048;
static constexpr int F_OX = 3072;
static constexpr int F_OXB = 4096;
static constexpr int F_WORDS = 4096 + 64 * 32 * 16;
static constexpr unsigned O_FL = 0;
static constexpr unsigned O_P2 = F_WORDS * 4;
static constexpr unsigned O_P1 = O_P2 + NSEG * 4096 * 4;
static constexpr unsigned O_ZEND = O_P1 + NSEG * 32 * 32 * 128 * 4;
static constexpr unsigned O_XN = O_ZEND;
static constexpr unsigned O_Y = O_XN + S_ * DM * 2;
static constexpr unsigned O_QKV = O_Y + S_ * DM * 2;
static constexpr unsigned O_H1 = O_QKV + S_ * NQKV * 2;
static constexpr unsigned O_G = O_H1 + S_ * FF * 2;
static constexpr unsigned O_KVM = O_G + S_ * DM * 2;
static constexpr unsigned O_END = O_KVM + NSEG * MM * 8192 * 2;

__device__ __forceinline__ void st8_sc1(u16* p, u32x2 v) {
  __hip_atomic_store((unsigned long long*)p, (unsigned long long)v[0] | ((unsigned long long)v[1] << 32), RLX_AGENT);
}
__device__ __forceinline__ void publish(unsigned* c1, unsigned* c2) {
  asm volatile("s_waitcnt vmcnt(0)" ::: "memory");
  __syncthreads();
  if (threadIdx.x == 0) {
    __hip_atomic_fetch_add(c1, 1u, RLX_AGENT);
    if (c2) __hip_atomic_fetch_add(c2, 1u, RLX_AGENT);
  }
}
template <int SLP = 4>
__device__ __forceinline__ void wait1(unsigned* c, unsigned target) {
  unsigned spins = 0;
  while (__hip_atomic_load(c, RLX_AGENT) < target) {
    __builtin_amdgcn_s_sleep(SLP);
    if (++spins > (1u << 22)) break;
  }
}
__device__ __forceinline__ void acquire_all() {
  if (threadIdx.x == 0) {
    __builtin_amdgcn_fence(__ATOMIC_ACQUIRE, "agent");
    asm volatile("s_waitcnt vmcnt(0)" ::: "memory");
  }
  __syncthreads();
}

struct EpiStore {
  u16* C; int ldc; int pad_;
  __device__ __forceinline__ void operator()(int m, int n, f32x4 a) const {
    u32x2 o; o[0] = pack2(a[0], a[1]); o[1] = pack2(a[2], a[3]);
    st8_sc1(C + (size_t)m * ldc + n, o);
  }
};
struct EpiGelu {
  u16* C; int ldc; int pad_; const u16* b;
  __device__ __forceinline__ void operator()(int m, int n, f32x4 a) const {
    float r[4];
#pragma unroll
    for (int i = 0; i < 4; ++i) {
      float v = rbf(rbf(a[i]) + bf2f(b[n + i]));
      float u = 0.7978845608028654f * (v + 0.044715f * v * v * v);
      r[i] = 0.5f * v * (1.f + tanhf(u));
    }
    u32x2 o; o[0] = pack2(r[0], r[1]); o[1] = pack2(r[2], r[3]);
    st8_sc1(C + (size_t)m * ldc + n, o);
  }
};
struct EpiSilu {
  u16* C; int ldc; int pad_; const u16* b;
  __device__ __forceinline__ void operator()(int m, int n, f32x4 a) const {
    float r[4];
#pragma unroll
    for (int i = 0; i < 4; ++i) {
      float v = rbf(rbf(a[i]) + bf2f(b[n + i]));
      r[i] = v / (1.f + __expf(-v));
    }
    u32x2 o; o[0] = pack2(r[0], r[1]); o[1] = pack2(r[2], r[3]);
    st8_sc1(C + (size_t)m * ldc + n, o);
  }
};

template <int BM, int BN, int BK>
struct GemmCfg {
  static constexpr int ASTR = BK * 2 + 16;
  static constexpr int BSTR = BN * 2 + 32;
  static constexpr int ABYTES = BM * ASTR, BBYTES = BK * BSTR;
  static constexpr int LDS = 2 * (ABYTES + BBYTES);
};

template <int BM, int BN, int BK, class Epi>
__device__ __forceinline__ void gemm_tile(char* smem, const u16* __restrict__ A, int lda, const u16* __restrict__ B, int ldb,
                                          int M, int N, int K, int bm, int bn, const Epi& epi) {
  using C = GemmCfg<BM, BN, BK>;
  constexpr int WM = BM / 2, WN = BN / 2, TM = WM / 16, TN = WN / 16;
  constexpr int ACH = BM * BK / 8 / 256;
  constexpr int BCH = BK * BN / 8 / 256;
  static_assert(ACH >= 1 && BCH >= 1, "tile too small");
  const int tid = threadIdx.x, w = tid >> 6, l = tid & 63, g = l >> 4, c16 = l & 15;
  const int wm = w >> 1, wn = w & 1;
  f32x4 acc[TM][TN];
#pragma unroll
  for (int i = 0; i < TM; ++i)
#pragma unroll
    for (int j = 0; j < TN; ++j) acc[i][j] = (f32x4){0, 0, 0, 0};
  u32x4 ra[ACH], rb[BCH];
  auto gload = [&](int k0) {
#pragma unroll
    for (int i = 0; i < ACH; ++i) {
      int ch = tid + i * 256, r = ch / (BK / 8), c = ch % (BK / 8);
      int k = k0 + c * 8;
      ra[i] = (k < K) ? *(const u32x4*)(A + (size_t)(bm + r) * lda + k) : (u32x4){0, 0, 0, 0};
    }
#pragma unroll
    for (int i = 0; i < BCH; ++i) {
      int ch = tid + i * 256, r = ch / (BN / 8), c = ch % (BN / 8);
      int k = k0 + r, n = bn + c * 8;
      rb[i] = (k < K && n < N) ? *(const u32x4*)(B + (size_t)k * ldb + n) : (u32x4){0, 0, 0, 0};
    }
  };
  auto lstore = [&](int buf) {
    char* sa = smem + buf * (C::ABYTES + C::BBYTES);
    char* sb = sa + C::ABYTES;
#pragma unroll
    for (int i = 0; i < ACH; ++i) {
      int ch = tid + i * 256, r = ch / (BK / 8), c = ch % (BK / 8);
      *(u32x4*)(sa + r * C::ASTR + c * 16) = ra[i];
    }
#pragma unroll
    for (int i = 0; i < BCH; ++i) {
      int ch = tid + i * 256, r = ch / (BN / 8), c = ch % (BN / 8);
      *(u32x4*)(sb + r * C::BSTR + ((c ^ (((r >> 3) & 1) * (BN / 16))) * 16)) = rb[i];
    }
  };
  const int nk = (K + BK - 1) / BK;
  gload(0);
  lstore(0);
  __syncthreads();
  for (int kt = 0; kt < nk; ++kt) {
    int buf = kt & 1;
    if (kt + 1 < nk) gload((kt + 1) * BK);
    const char* sa = smem + buf * (C::ABYTES + C::BBYTES);
    const char* sb = sa + C::ABYTES;
#pragma unroll
    for (int ks = 0; ks < BK / 32; ++ks) {
      bf16x8 af[TM], bfr[TN];
#pragma unroll
      for (int i = 0; i < TM; ++i)
        af[i] = *(const bf16x8*)(sa + (wm * WM + i * 16 + c16) * C::ASTR + (ks * 32 + g * 8) * 2);
#pragma unroll
      for (int j = 0; j < TN; ++j) {
        int q = c16 >> 2, p = c16 & 3;
        int col = wn * WN + j * 16 + p * 4;
        s16x4 lo, hi;
        {
          int r = ks * 32 + g * 8 + q;
          lo = __builtin_amdgcn_ds_read_tr16_b64_v4i16((LAS s16x4*)(sb + r * C::BSTR + ((col * 2) ^ (((r >> 3) & 1) * BN))));
          r += 4;
          hi = __builtin_amdgcn_ds_read_tr16_b64_v4i16((LAS s16x4*)(sb + r * C::BSTR + ((col * 2) ^ (((r >> 3) & 1) * BN))));
        }
        typedef __attribute__((ext_vector_type(8))) short s16x8;
        s16x8 t = __builtin_shufflevector(lo, hi, 0, 1, 2, 3, 4, 5, 6, 7);
        bfr[j] = __builtin_bit_cast(bf16x8, t);
      }
#pragma unroll
      for (int i = 0; i < TM; ++i)
#pragma unroll
        for (int j = 0; j < TN; ++j) acc[i][j] = __builtin_amdgcn_mfma_f32_16x16x32_bf16(bfr[j], af[i], acc[i][j], 0, 0, 0);
    }
    if (kt + 1 < nk) lstore(buf ^ 1);
    __syncthreads();
  }
#pragma unroll
  for (int i = 0; i < TM; ++i)
#pragma unroll
    for (int j = 0; j < TN; ++j) {
      int m = bm + wm * WM + i * 16 + c16, n = bn + wn * WN + j * 16 + g * 4;
      if (n < N) epi(m, n, acc[i][j]);
    }
}

__device__ __forceinline__ unsigned swz(unsigned row, unsigned ch) {
  return 256u * row + 16u * (ch ^ (((row & 3) << 2) | ((row >> 2) & 3)));
}

static constexpr int L_WO = 0;
static constexpr int L_V = 131072;
static constexpr int L_K = L_V + 40 * 256;
static constexpr int L_Q = L_K + 40 * 256;
static constexpr int L_MEM = L_Q + 8 * 256;
static constexpr int L_END = L_MEM + 8704;
static_assert(L_END <= 163840, "lds");

#define STAMP(i)

__device__ __forceinline__ void recurrence(const Params& p, char* smem) {
  const u16* __restrict__ Wq = p.Wq; const u16* __restrict__ Wk = p.Wk; const u16* __restrict__ Wv = p.Wv;
  const u16* __restrict__ Wo = p.Wo; const u16* __restrict__ mem0 = p.mem0;
  unsigned long long* stamps = p.stamps;
  const int h = blockIdx.x;
  const __amdgpu_buffer_rsrc_t wr = __builtin_amdgcn_make_buffer_rsrc(p.fl, 0, O_END, 0x00020000);
  unsigned* const fl = p.fl;
  typedef __attribute__((ext_vector_type(8))) short s16x8;

  bf16x8 wf[3][2][16];
  {
    const int tid = threadIdx.x, w = tid >> 6, l = tid & 63, g = l >> 4, c16 = l & 15;
    const int q4 = c16 >> 2, p4 = c16 & 3;
#pragma unroll
    for (int m = 0; m < 3; ++m) {
      const u16* W = m == 0 ? Wq : (m == 1 ? Wk : Wv);
      for (int i0 = 0; i0 < 32; i0 += 8) {
        u32x4 v[8];
#pragma unroll
        for (int i = 0; i < 8; ++i) {
          int ch = tid + (i0 + i) * 256, r = ch >> 4, c = ch & 15;
          v[i] = *(const u32x4*)(W + (size_t)r * 4096 + h * 128 + c * 8);
        }
#pragma unroll
        for (int i = 0; i < 8; ++i) {
          int ch = tid + (i0 + i) * 256, r = ch >> 4, c = ch & 15;
          *(u32x4*)(smem + swz(r, c)) = v[i];
        }
      }
      __syncthreads();
#pragma unroll
      for (int c = 0; c < 2; ++c)
#pragma unroll
        for (int t = 0; t < 16; ++t) {
          int ct = 2 * w + c;
          int r0 = 32 * t + 8 * g + q4;
          s16x4 lo = __builtin_amdgcn_ds_read_tr16_b64_v4i16((LAS s16x4*)(smem + swz(r0, 2 * ct + (p4 >> 1)) + 8 * (p4 & 1)));
          s16x4 hi = __builtin_amdgcn_ds_read_tr16_b64_v4i16((LAS s16x4*)(smem + swz(r0 + 4, 2 * ct + (p4 >> 1)) + 8 * (p4 & 1)));
          s16x8 tt = __builtin_shufflevector(lo, hi, 0, 1, 2, 3, 4, 5, 6, 7);
          wf[m][c][t] = __builtin_bit_cast(bf16x8, tt);
        }
      __syncthreads();
    }
    for (int t = 0; t < 4; ++t) {
      char* st = smem + 131072;
      u32x4 v[8];
#pragma unroll
      for (int i = 0; i < 8; ++i) {
        int ch = tid + i * 256, r = ch >> 6, c = ch & 63;
        v[i] = *(const u32x4*)(Wo + (size_t)(h * 128 + t * 32 + r) * 512 + c * 8);
      }
#pragma unroll
      for (int i = 0; i < 8; ++i) {
        int ch = tid + i * 256, r = ch >> 6, c = ch & 63;
        *(u32x4*)(st + (c >> 4) * 8192 + swz(r, c & 15)) = v[i];
      }
      __syncthreads();
#pragma unroll
      for (int j = 0; j < 8; ++j) {
        int r0 = 8 * g + q4;
        s16x4 lo = __builtin_amdgcn_ds_read_tr16_b64_v4i16((LAS s16x4*)(st + w * 8192 + swz(r0, 2 * j + (p4 >> 1)) + 8 * (p4 & 1)));
        s16x4 hi = __builtin_amdgcn_ds_read_tr16_b64_v4i16((LAS s16x4*)(st + w * 8192 + swz(r0 + 4, 2 * j + (p4 >> 1)) + 8 * (p4 & 1)));
        s16x8 tt = __builtin_shufflevector(lo, hi, 0, 1, 2, 3, 4, 5, 6, 7);
        *(s16x8*)(smem + L_WO + ((w * 8 + j) * 4 + t) * 1024 + l * 16) = tt;
      }
      __syncthreads();
    }
    if (l == 0) { wait1(fl + F_TILE + h, 1u); wait1(fl + F_TILE + 32 + h, 1u); }
    asm volatile("" ::: "memory");
#pragma unroll
    for (int i = 0; i < 2; ++i) {
      int ch = tid + i * 256, r = ch >> 4, c = ch & 15;
      unsigned off = (unsigned)(r * NQKV + 4096 + h * 128 + c * 8) * 2;
      *(u32x4*)(smem + L_K + swz(8 + r, c)) = __builtin_amdgcn_raw_buffer_load_b128(wr, O_QKV + off, 0, 16);
      *(u32x4*)(smem + L_V + swz(8 + r, c)) = __builtin_amdgcn_raw_buffer_load_b128(wr, O_QKV + off + 8192, 0, 16);
    }
  }

  const float scale = 0.08838834764831845f;
  u32x4 pk[2], pv[2];

  for (int s = 0; s < NSEG; ++s) {
    int tid = threadIdx.x;
    asm volatile("" : "+v"(tid));
    const int w = tid >> 6, l = tid & 63, g = l >> 4, c16 = l & 15;
    const int q4 = c16 >> 2, p4 = c16 & 3;
    unsigned spins = 0;
    STAMP(0)
    if (s == 0) {
#pragma unroll
      for (int i = 0; i < 2; ++i) {
        int ch = tid + i * 256, r = ch >> 6, c = ch & 63;
        u32x4 v = *(const u32x4*)(mem0 + r * 512 + c * 8);
        *(u32x4*)(smem + L_MEM + r * 1088 + c * 16) = v;
      }
    } else {
      u32x4 a[4];
      while (true) {
        unsigned ok = 1u;
        asm volatile("" ::: "memory");
#pragma unroll
        for (int i = 0; i < 4; ++i) a[i] = __builtin_amdgcn_raw_buffer_load_b128(wr, O_P2 + (unsigned)(s * 4096) * 4 + (i * 256 + tid) * 16, 0, 16);
#pragma unroll
        for (int i = 0; i < 4; ++i) ok &= a[i][0] & a[i][1] & a[i][2] & a[i][3];
        if (__all(ok & 1u) || ++spins > (1u << 20)) break;
      }
#pragma unroll
      for (int i = 0; i < 4; ++i) {
        int ch = i * 256 + tid, r = ch >> 5, qy = (ch >> 2) & 7, qd = ch & 3;
        u32x2 v; v[0] = pack2(__uint_as_float(a[i][0]), __uint_as_float(a[i][1])); v[1] = pack2(__uint_as_float(a[i][2]), __uint_as_float(a[i][3]));
        *(u32x2*)(smem + L_MEM + qy * 1088 + (16 * r + 4 * qd) * 2) = v;
      }
    }
    __syncthreads();
    STAMP(1)
    f32x4 acc[3][2];
#pragma unroll
    for (int m = 0; m < 3; ++m)
#pragma unroll
      for (int c = 0; c < 2; ++c) acc[m][c] = (f32x4){0, 0, 0, 0};
    {
      const char* mb = smem + L_MEM + (c16 & 7) * 1088 + g * 16;
      bf16x8 mf[16];
#pragma unroll
      for (int t = 0; t < 3; ++t) mf[t] = *(const bf16x8*)(mb + t * 64);
#pragma unroll
      for (int t = 0; t < 16; ++t) {
        if (t + 3 < 16) mf[t + 3] = *(const bf16x8*)(mb + (t + 3) * 64);
#pragma unroll
        for (int m = 0; m < 3; ++m)
#pragma unroll
          for (int c = 0; c < 2; ++c) acc[m][c] = __builtin_amdgcn_mfma_f32_16x16x32_bf16(wf[m][c][t], mf[t], acc[m][c], 0, 0, 0);
        __builtin_amdgcn_sched_barrier(0);
      }
    }
    if (c16 < 8) {
      const unsigned sw = (((c16 & 3) << 2) | ((c16 >> 2) & 3));
#pragma unroll
      for (int c = 0; c < 2; ++c) {
        const int chunk = 4 * w + 2 * c + (g >> 1);
        const unsigned off = 256u * c16 + 16u * (chunk ^ sw) + (g & 1) * 8;
        u32x2 vq, vk, vv;
        vq[0] = pack2(acc[0][c][0], acc[0][c][1]); vq[1] = pack2(acc[0][c][2], acc[0][c][3]);
        vk[0] = pack2(acc[1][c][0], acc[1][c][1]); vk[1] = pack2(acc[1][c][2], acc[1][c][3]);
        vv[0] = pack2(acc[2][c][0], acc[2][c][1]); vv[1] = pack2(acc[2][c][2], acc[2][c][3]);
        *(u32x2*)(smem + L_Q + off) = vq;
        *(u32x2*)(smem + L_K + off) = vk;
        *(u32x2*)(smem + L_V + off) = vv;
      }
    }
    __syncthreads();
    STAMP(2)
    f32x4 sc[3];
    s16x4 va[2][3];
    {
      bf16x8 qf[4], kf[3][4];
#pragma unroll
      for (int t = 0; t < 4; ++t) {
        qf[t] = *(const bf16x8*)(smem + L_Q + swz(c16 & 7, 4 * t + g));
        kf[0][t] = *(const bf16x8*)(smem + L_K + swz(c16, 4 * t + g));
      }
#pragma unroll
      for (int t = 0; t < 4; ++t) kf[1][t] = *(const bf16x8*)(smem + L_K + swz(16 + c16, 4 * t + g));
      __builtin_amdgcn_sched_barrier(0);
      sc[0] = (f32x4){0, 0, 0, 0};
#pragma unroll
      for (int t = 0; t < 4; ++t) sc[0] = __builtin_amdgcn_mfma_f32_16x16x32_bf16(kf[0][t], qf[t], sc[0], 0, 0, 0);
      __builtin_amdgcn_sched_barrier(0);
#pragma unroll
      for (int t = 0; t < 4; ++t) kf[2][t] = *(const bf16x8*)(smem + L_K + swz(32 + c16, 4 * t + g));
      sc[1] = (f32x4){0, 0, 0, 0};
#pragma unroll
      for (int t = 0; t < 4; ++t) sc[1] = __builtin_amdgcn_mfma_f32_16x16x32_bf16(kf[1][t], qf[t], sc[1], 0, 0, 0);
      __builtin_amdgcn_sched_barrier(0);
#pragma unroll
      for (int c = 0; c < 2; ++c) {
        int ch = 2 * (2 * w + c) + (p4 >> 1);
#pragma unroll
        for (int kt = 0; kt < 3; ++kt)
          va[c][kt] = __builtin_amdgcn_ds_read_tr16_b64_v4i16((LAS s16x4*)(smem + L_V + swz(16 * kt + 4 * g + q4, ch) + 8 * (p4 & 1)));
      }
      sc[2] = (f32x4){0, 0, 0, 0};
#pragma unroll
      for (int t = 0; t < 4; ++t) sc[2] = __builtin_amdgcn_mfma_f32_16x16x32_bf16(kf[2][t], qf[t], sc[2], 0, 0, 0);
    }
    __builtin_amdgcn_sched_barrier(0);
    if (s + 1 < NSEG) {
      if (((s + 1) & 3) == 0) {
        const int rb = (s + 1) >> 2;
        if (l == 0) { wait1(fl + F_TILE + rb * 64 + h, 1u); wait1(fl + F_TILE + rb * 64 + 32 + h, 1u); }
        asm volatile("" ::: "memory");
      }
#pragma unroll
      for (int i = 0; i < 2; ++i) {
        int ch = tid + i * 256, r = ch >> 4, c = ch & 15;
        unsigned off = (unsigned)(((s + 1) * SEG + r) * NQKV + 4096 + h * 128 + c * 8) * 2;
        pk[i] = __builtin_amdgcn_raw_buffer_load_b128(wr, O_QKV + off, 0, 16);
        pv[i] = __builtin_amdgcn_raw_buffer_load_b128(wr, O_QKV + off + 8192, 0, 16);
      }
    }
    float mx = -3.0e38f;
#pragma unroll
    for (int kt = 0; kt < 3; ++kt)
#pragma unroll
      for (int r = 0; r < 4; ++r) {
        float v = rbf(rbf(sc[kt][r]) * scale);
        if (kt == 2 && g >= 2) v = -3.0e38f;
        sc[kt][r] = v;
        mx = fmaxf(mx, v);
      }
    {
      u32x2 r = __builtin_amdgcn_permlane16_swap(__float_as_uint(mx), __float_as_uint(mx), false, false);
      mx = fmaxf(__uint_as_float(r[0]), __uint_as_float(r[1]));
      r = __builtin_amdgcn_permlane32_swap(__float_as_uint(mx), __float_as_uint(mx), false, false);
      mx = fmaxf(__uint_as_float(r[0]), __uint_as_float(r[1]));
    }
    float sum = 0.f;
#pragma unroll
    for (int kt = 0; kt < 3; ++kt)
#pragma unroll
      for (int r = 0; r < 4; ++r) {
        float e = (kt == 2 && g >= 2) ? 0.f : __expf(sc[kt][r] - mx);
        sc[kt][r] = e;
        sum += e;
      }
    {
      u32x2 r = __builtin_amdgcn_permlane16_swap(__float_as_uint(sum), __float_as_uint(sum), false, false);
      sum = __uint_as_float(r[0]) + __uint_as_float(r[1]);
      r = __builtin_amdgcn_permlane32_swap(__float_as_uint(sum), __float_as_uint(sum), false, false);
      sum = __uint_as_float(r[0]) + __uint_as_float(r[1]);
    }
    float inv = __builtin_amdgcn_rcpf(sum);
    u32x4 pb0, pb1;
    pb0[0] = pack2(sc[0][0] * inv, sc[0][1] * inv); pb0[1] = pack2(sc[0][2] * inv, sc[0][3] * inv);
    pb0[2] = pack2(sc[1][0] * inv, sc[1][1] * inv); pb0[3] = pack2(sc[1][2] * inv, sc[1][3] * inv);
    pb1[0] = pack2(sc[2][0] * inv, sc[2][1] * inv); pb1[1] = pack2(sc[2][2] * inv, sc[2][3] * inv);
    pb1[2] = 0; pb1[3] = 0;
    bf16x8 pf0 = __builtin_bit_cast(bf16x8, pb0), pf1 = __builtin_bit_cast(bf16x8, pb1);
    {
      f32x4 o[2];
#pragma unroll
      for (int c = 0; c < 2; ++c) {
        s16x8 v0 = __builtin_shufflevector(va[c][0], va[c][1], 0, 1, 2, 3, 4, 5, 6, 7);
        o[c] = __builtin_amdgcn_mfma_f32_16x16x32_bf16(__builtin_bit_cast(bf16x8, v0), pf0, (f32x4){0, 0, 0, 0}, 0, 0, 0);
      }
#pragma unroll
      for (int c = 0; c < 2; ++c) {
        s16x8 v1 = __builtin_shufflevector(va[c][2], va[c][2], 0, 1, 2, 3, 4, 5, 6, 7);
        o[c] = __builtin_amdgcn_mfma_f32_16x16x32_bf16(__builtin_bit_cast(bf16x8, v1), pf1, o[c], 0, 0, 0);
      }
      if (c16 < 8) {
        const unsigned sw = (((c16 & 3) << 2) | ((c16 >> 2) & 3));
#pragma unroll
        for (int c = 0; c < 2; ++c) {
          const int chunk = 4 * w + 2 * c + (g >> 1);
          u32x2 v; v[0] = pack2(o[c][0], o[c][1]); v[1] = pack2(o[c][2], o[c][3]);
          *(u32x2*)(smem + L_MEM + 256u * c16 + 16u * (chunk ^ sw) + (g & 1) * 8) = v;
        }
      }
    }
    __syncthreads();
    STAMP(3)
    if (s + 1 < NSEG) {
      bf16x8 of[4];
#pragma unroll
      for (int t = 0; t < 4; ++t) of[t] = *(const bf16x8*)(smem + L_MEM + swz(c16 & 7, 4 * t + g));
      const char* wb = smem + L_WO + w * 32768 + l * 16;
      f32x4 d[8];
      bf16x8 bw[32];
      constexpr int PD = 6;
#pragma unroll
      for (int u = 0; u < PD; ++u) bw[u] = *(const bf16x8*)(wb + (u & 7) * 4096 + (u >> 3) * 1024);
#pragma unroll
      for (int u = 0; u < 32; ++u) {
        if (u + PD < 32) bw[u + PD] = *(const bf16x8*)(wb + ((u + PD) & 7) * 4096 + ((u + PD) >> 3) * 1024);
        const int t = u >> 3, j = u & 7;
        if (t == 0) d[j] = __builtin_amdgcn_mfma_f32_16x16x32_bf16(bw[u], of[t], (f32x4){0, 0, 0, 0}, 0, 0, 0);
        else d[j] = __builtin_amdgcn_mfma_f32_16x16x32_bf16(bw[u], of[t], d[j], 0, 0, 0);
        __builtin_amdgcn_sched_barrier(0);
      }
      if (c16 < 8) {
        const unsigned base = O_P1 + (unsigned)((((s + 1) * 32 + 8 * w) * 32 + h) * 128 + c16 * 16 + 4 * g) * 4;
#pragma unroll
        for (int j = 0; j < 8; ++j) {
          u32x4 v;
#pragma unroll
          for (int r = 0; r < 4; ++r) v[r] = __float_as_uint(d[j][r]) | 1u;
          __builtin_amdgcn_raw_buffer_store_b128(v, wr, base + j * (32 * 128 * 4), 0, 16);
        }
      }
      STAMP(4)
      {
        int r = tid >> 5, c = tid & 31;
        u32x4 v = *(const u32x4*)(smem + (c < 16 ? L_K : L_V) + swz(r, c & 15));
        __builtin_amdgcn_raw_buffer_store_b128(v, wr, O_KVM + (unsigned)((s * MM + r) * 8192 + (c >> 4) * 4096 + h * 128 + (c & 15) * 8) * 2, 0, 16);
#pragma unroll
        for (int i = 0; i < 2; ++i) {
          int ch = tid + i * 256, rr = ch >> 4, cc = ch & 15;
          *(u32x4*)(smem + L_K + swz(8 + rr, cc)) = pk[i];
          *(u32x4*)(smem + L_V + swz(8 + rr, cc)) = pv[i];
        }
      }
      f32x4 t4;
      while (true) {
        u32x4 a[4]; unsigned ok = 1u;
        asm volatile("" ::: "memory");
#pragma unroll
        for (int i = 0; i < 4; ++i)
          a[i] = __builtin_amdgcn_raw_buffer_load_b128(wr, O_P1 + (unsigned)((((s + 1) * 32 + h) * 32 + i * 8 + (l >> 3)) * 128 + 32 * w + (l & 7) * 4) * 4, 0, 16);
        t4 = (f32x4){0, 0, 0, 0};
#pragma unroll
        for (int i = 0; i < 4; ++i) {
          ok &= a[i][0] & a[i][1] & a[i][2] & a[i][3];
#pragma unroll
          for (int r = 0; r < 4; ++r) t4[r] += __uint_as_float(a[i][r]);
        }
        if (__all(ok & 1u) || ++spins > (1u << 20)) break;
      }
      STAMP(5)
      if (l == 0) __hip_atomic_fetch_add(fl + F_M + s * 16, 1u, RLX_AGENT);
      u32x4 v;
#pragma unroll
      for (int r = 0; r < 4; ++r) {
        float x = t4[r];
        x += __uint_as_float(__builtin_amdgcn_update_dpp(0, __float_as_uint(x), 0x128  , 0xf, 0xf, false));
        u32x2 q = __builtin_amdgcn_permlane16_swap(__float_as_uint(x), __float_as_uint(x), false, false);
        x = __uint_as_float(q[0]) + __uint_as_float(q[1]);
        q = __builtin_amdgcn_permlane32_swap(__float_as_uint(x), __float_as_uint(x), false, false);
        x = __uint_as_float(q[0]) + __uint_as_float(q[1]);
        v[r] = __float_as_uint(x) | 1u;
      }
      if (l < 8) __builtin_amdgcn_raw_buffer_store_b128(v, wr, O_P2 + (unsigned)((s + 1) * 4096 + h * 128 + 32 * w + l * 4) * 4, 0, 16);
      STAMP(6)
    } else {
      int r = tid >> 5, c = tid & 31;
      u32x4 v = *(const u32x4*)(smem + (c < 16 ? L_K : L_V) + swz(r, c & 15));
      __builtin_amdgcn_raw_buffer_store_b128(v, wr, O_KVM + (unsigned)((s * MM + r) * 8192 + (c >> 4) * 4096 + h * 128 + (c & 15) * 8) * 2, 0, 16);
      asm volatile("s_waitcnt vmcnt(0)" ::: "memory");
      if (l == 0) __hip_atomic_fetch_add(fl + F_M + s * 16, 1u, RLX_AGENT);
    }
  }
}

__device__ __forceinline__ void task_attn(const Params& p, char* smem, int s, int hg) {
  const int tid = threadIdx.x, w = tid >> 6, l = tid & 63, g = l >> 4, c16 = l & 15;
  const int q4 = c16 >> 2, p4 = c16 & 3;
  typedef __attribute__((ext_vector_type(8))) short s16x8;
  const u16* __restrict__ qkv = p.qkv;
  const u16* __restrict__ kvm = p.kvm;
  const float scale = 0.08838834764831845f;
  char* sv = smem + w * 48 * 256;
  const int h = hg * 4 + w;
  u32x4 vst[12];
#pragma unroll
  for (int i = 0; i < 12; ++i) {
    int ch = l + i * 64, r = ch >> 4, c = ch & 15;
    u32x4 v = (u32x4){0, 0, 0, 0};
    if (r < 8) v = *(const u32x4*)(kvm + (size_t)(s * MM + r) * 8192 + 4096 + h * 128 + c * 8);
    else if (r < 40) v = *(const u32x4*)(qkv + (size_t)(s * SEG + r - 8) * NQKV + 8192 + h * 128 + c * 8);
    vst[i] = v;
  }
  bf16x8 qf[4][2], kf[4][3];
#pragma unroll
  for (int t = 0; t < 4; ++t) {
#pragma unroll
    for (int qt = 0; qt < 2; ++qt)
      qf[t][qt] = *(const bf16x8*)(qkv + (size_t)(s * SEG + 16 * qt + c16) * NQKV + h * 128 + 32 * t + 8 * g);
#pragma unroll
    for (int kt = 0; kt < 3; ++kt) {
      int key = 16 * kt + c16;
      const u16* src = key < 8 ? kvm + (size_t)(s * MM + key) * 8192 + h * 128
                               : qkv + (size_t)(s * SEG + (key < 40 ? key - 8 : 0)) * NQKV + 4096 + h * 128;
      kf[t][kt] = *(const bf16x8*)(src + 32 * t + 8 * g);
    }
  }
#pragma unroll
  for (int i = 0; i < 12; ++i) {
    int ch = l + i * 64, r = ch >> 4, c = ch & 15;
    *(u32x4*)(sv + swz(r, c)) = vst[i];
  }
  f32x4 sc[3][2];
#pragma unroll
  for (int kt = 0; kt < 3; ++kt)
#pragma unroll
    for (int qt = 0; qt < 2; ++qt) sc[kt][qt] = (f32x4){0, 0, 0, 0};
#pragma unroll
  for (int t = 0; t < 4; ++t)
#pragma unroll
    for (int kt = 0; kt < 3; ++kt)
#pragma unroll
      for (int qt = 0; qt < 2; ++qt) sc[kt][qt] = __builtin_amdgcn_mfma_f32_16x16x32_bf16(kf[t][kt], qf[t][qt], sc[kt][qt], 0, 0, 0);
  bf16x8 pf0[2], pf1[2];
#pragma unroll
  for (int qt = 0; qt < 2; ++qt) {
    int qi = 8 + 16 * qt + c16;
    float mx = -3.0e38f;
#pragma unroll
    for (int kt = 0; kt < 3; ++kt)
#pragma unroll
      for (int r = 0; r < 4; ++r) {
        int key = 16 * kt + 4 * g + r;
        float v = rbf(rbf(sc[kt][qt][r]) * scale);
        if (key > qi) v = -1e9f;
        if (key >= 40) v = -3.0e38f;
        sc[kt][qt][r] = v;
        mx = fmaxf(mx, v);
      }
    mx = fmaxf(mx, __shfl_xor(mx, 16));
    mx = fmaxf(mx, __shfl_xor(mx, 32));
    float sum = 0.f;
#pragma unroll
    for (int kt = 0; kt < 3; ++kt)
#pragma unroll
      for (int r = 0; r < 4; ++r) {
        int key = 16 * kt + 4 * g + r;
        float e = (key >= 40) ? 0.f : __expf(sc[kt][qt][r] - mx);
        sc[kt][qt][r] = e;
        sum += e;
      }
    sum += __shfl_xor(sum, 16);
    sum += __shfl_xor(sum, 32);
    float inv = 1.f / sum;
    u32x4 b0, b1;
    b0[0] = pack2(sc[0][qt][0] * inv, sc[0][qt][1] * inv); b0[1] = pack2(sc[0][qt][2] * inv, sc[0][qt][3] * inv);
    b0[2] = pack2(sc[1][qt][0] * inv, sc[1][qt][1] * inv); b0[3] = pack2(sc[1][qt][2] * inv, sc[1][qt][3] * inv);
    b1[0] = pack2(sc[2][qt][0] * inv, sc[2][qt][1] * inv); b1[1] = pack2(sc[2][qt][2] * inv, sc[2][qt][3] * inv);
    b1[2] = 0; b1[3] = 0;
    pf0[qt] = __builtin_bit_cast(bf16x8, b0);
    pf1[qt] = __builtin_bit_cast(bf16x8, b1);
  }
  __builtin_amdgcn_s_waitcnt(0xc07f);
  __builtin_amdgcn_wave_barrier();
#pragma unroll
  for (int c = 0; c < 8; ++c) {
    int ch = 2 * c + (p4 >> 1);
    s16x4 a0 = __builtin_amdgcn_ds_read_tr16_b64_v4i16((LAS s16x4*)(sv + swz(4 * g + q4, ch) + 8 * (p4 & 1)));
    s16x4 a1 = __builtin_amdgcn_ds_read_tr16_b64_v4i16((LAS s16x4*)(sv + swz(16 + 4 * g + q4, ch) + 8 * (p4 & 1)));
    s16x4 a2 = __builtin_amdgcn_ds_read_tr16_b64_v4i16((LAS s16x4*)(sv + swz(32 + 4 * g + q4, ch) + 8 * (p4 & 1)));
    s16x8 v0 = __builtin_shufflevector(a0, a1, 0, 1, 2, 3, 4, 5, 6, 7);
    s16x8 v1 = __builtin_shufflevector(a2, a2, 0, 1, 2, 3, 4, 5, 6, 7);
#pragma unroll
    for (int qt = 0; qt < 2; ++qt) {
      f32x4 o = (f32x4){0, 0, 0, 0};
      o = __builtin_amdgcn_mfma_f32_16x16x32_bf16(__builtin_bit_cast(bf16x8, v0), pf0[qt], o, 0, 0, 0);
      o = __builtin_amdgcn_mfma_f32_16x16x32_bf16(__builtin_bit_cast(bf16x8, v1), pf1[qt], o, 0, 0, 0);
      u32x2 v; v[0] = pack2(o[0], o[1]); v[1] = pack2(o[2], o[3]);
      st8_sc1(p.ox + (size_t)(s * SEG + 16 * qt + c16) * 4096 + h * 128 + 16 * c + 4 * g, v);
    }
  }
}

static constexpr int E_WO = 0;
static constexpr int E_RED = 131072;
__device__ __forceinline__ void load_wo_slice(const Params& p, char* smem, int cb) {
  const int tid = threadIdx.x, w = tid >> 6, l = tid & 63, g = l >> 4, c16 = l & 15;
  const int q4 = c16 >> 2, p4 = c16 & 3;
  typedef __attribute__((ext_vector_type(8))) short s16x8;
  char* st = smem + E_RED;
  for (int r8 = 0; r8 < 8; ++r8) {
    u32x4 v[4];
#pragma unroll
    for (int i = 0; i < 4; ++i) {
      int ch = tid + i * 256, r = ch >> 1, c = ch & 1;
      v[i] = *(const u32x4*)(p.Wo + (size_t)(r8 * 512 + r) * DM + cb * 16 + c * 8);
    }
#pragma unroll
    for (int i = 0; i < 4; ++i) {
      int ch = tid + i * 256, r = ch >> 1, c = ch & 1;
      *(u32x4*)(st + r * 32 + c * 16) = v[i];
    }
    __syncthreads();
#pragma unroll
    for (int t = 0; t < 4; ++t) {
      int ks = 4 * w + t;
      int r0 = 32 * ks + 8 * g + q4;
      s16x4 lo = __builtin_amdgcn_ds_read_tr16_b64_v4i16((LAS s16x4*)(st + r0 * 32 + p4 * 8));
      s16x4 hi = __builtin_amdgcn_ds_read_tr16_b64_v4i16((LAS s16x4*)(st + (r0 + 4) * 32 + p4 * 8));
      s16x8 tt = __builtin_shufflevector(lo, hi, 0, 1, 2, 3, 4, 5, 6, 7);
      *(s16x8*)(smem + E_WO + (r8 * 16 + ks) * 1024 + l * 16) = tt;
    }
    __syncthreads();
  }
}
__device__ __forceinline__ void task_out(const Params& p, char* smem, int s, int cb) {
  const int tid = threadIdx.x, w = tid >> 6, l = tid & 63, g = l >> 4, c16 = l & 15;
  f32x4 acc[2];
  acc[0] = (f32x4){0, 0, 0, 0}; acc[1] = (f32x4){0, 0, 0, 0};
  const u16* a0 = p.ox + (size_t)(s * SEG + c16) * 4096 + w * 1024 + 8 * g;
  const u16* a1 = a0 + 16 * 4096;
  const char* wb = smem + E_WO + w * 32768 + l * 16;
#pragma unroll 8
  for (int t = 0; t < 32; ++t) {
    bf16x8 x0 = *(const bf16x8*)(a0 + 32 * t), x1 = *(const bf16x8*)(a1 + 32 * t);
    bf16x8 wf = *(const bf16x8*)(wb + t * 1024);
    acc[0] = __builtin_amdgcn_mfma_f32_16x16x32_bf16(wf, x0, acc[0], 0, 0, 0);
    acc[1] = __builtin_amdgcn_mfma_f32_16x16x32_bf16(wf, x1, acc[1], 0, 0, 0);
  }
  *(f32x4*)(smem + E_RED + (w * 2 + 0) * 1024 + l * 16) = acc[0];
  *(f32x4*)(smem + E_RED + (w * 2 + 1) * 1024 + l * 16) = acc[1];
  __syncthreads();
  if (w < 2) {
    f32x4 a = *(f32x4*)(smem + E_RED + w * 1024 + l * 16);
#pragma unroll
    for (int i = 1; i < 4; ++i) a += *(f32x4*)(smem + E_RED + (i * 2 + w) * 1024 + l * 16);
    int m = s * SEG + 16 * w + c16, n = cb * 16 + 4 * g;
    u32x2 gg = *(const u32x2*)(p.g + (size_t)m * DM + n), xx = *(const u32x2*)(p.x + (size_t)m * DM + n);
    float gf[4] = {bf2f(gg[0] & 0xffff), bf2f(gg[0] >> 16), bf2f(gg[1] & 0xffff), bf2f(gg[1] >> 16)};
    float xf[4] = {bf2f(xx[0] & 0xffff), bf2f(xx[0] >> 16), bf2f(xx[1] & 0xffff), bf2f(xx[1] >> 16)};
    float r[4];
#pragma unroll
    for (int i = 0; i < 4; ++i) r[i] = xf[i] + rbf(rbf(a[i]) * gf[i]);
    u32x2 o; o[0] = pack2(r[0], r[1]); o[1] = pack2(r[2], r[3]);
    *(u32x2*)(p.out + (size_t)m * DM + n) = o;
  }
  __syncthreads();
}

__device__ __forceinline__ void worker(const Params& p, char* smem) {
  constexpr int NWK = 224;
  const int wid = blockIdx.x - NH;
  for (int t = wid; t < 1024; t += NWK) {
    int rb = t >> 6, ct = t & 63;
    const u16* B = ct < 32 ? p.Wk : p.Wv;
    EpiStore epi{p.qkv + 4096 + (ct >> 5) * 4096, NQKV, 0};
    gemm_tile<128, 128, 64, EpiStore>(smem, p.xn, DM, B, 4096, S_, 4096, DM, rb * 128, (ct & 31) * 128, epi);
    publish(p.fl + F_TILE + t, p.fl + F_QKV + rb * 16);
  }
  for (int t = (wid + 224 - (1024 % NWK)) % NWK; t < 176; t += NWK) {
    int rb = t / 11, ct = t % 11;
    EpiGelu epi{p.h1, FF, 0, p.b1};
    gemm_tile<128, 128, 64, EpiGelu>(smem, p.y, DM, p.w1, FF, S_, FF, DM, rb * 128, ct * 128, epi);
    publish(p.fl + F_H + rb * 16, nullptr);
  }
  for (int t = (wid + 48) % NWK; t < 512; t += NWK) {
    int rb = t >> 5, ct = t & 31;
    EpiStore epi{p.qkv, NQKV, 0};
    gemm_tile<128, 128, 64, EpiStore>(smem, p.xn, DM, p.Wq, 4096, S_, 4096, DM, rb * 128, ct * 128, epi);
    publish(p.fl + F_QKV + rb * 16, nullptr);
  }
  for (int t = (wid + 120) % NWK; t < 64; t += NWK) {
    int rb = t >> 2, ct = t & 3;
    if (threadIdx.x == 0) wait1<64>(p.fl + F_H + rb * 16, 11u);
    acquire_all();
    EpiSilu epi{p.g, DM, 0, p.b2};
    gemm_tile<128, 128, 64, EpiSilu>(smem, p.h1, FF, p.w2, DM, S_, DM, FF, rb * 128, ct * 128, epi);
    publish(p.fl + F_G + rb * 16, nullptr);
  }
  if (wid < 32) {
    for (int s = wid >> 3; s < NSEG; s += 4) {
      if (threadIdx.x == 0) {
        wait1<64>(p.fl + F_QKV + (s >> 2) * 16, 96u);
        wait1<64>(p.fl + F_M + s * 16, 128u);
      }
      acquire_all();
      task_attn(p, smem, s, wid & 7);
      asm volatile("s_waitcnt vmcnt(0)" ::: "memory");
      __syncthreads();
      if (threadIdx.x == 0) {
        unsigned old = __hip_atomic_fetch_add(p.fl + F_OX + s * 16, 1u, RLX_AGENT);
        if (old == 7u)
          for (int c = 0; c < 32; ++c) __hip_atomic_store(p.fl + F_OXB + (s * 32 + c) * 16, 1u, RLX_AGENT);
      }
    }
  } else {
    const int j = wid - 32, cb = j & 31, grp = j >> 5;
    load_wo_slice(p, smem, cb);
    for (int s = grp; s < NSEG; s += 6) {
      if (threadIdx.x == 0) {
        wait1<64>(p.fl + F_G + (s >> 2) * 16, 4u);
        wait1<64>(p.fl + F_OXB + (s * 32 + cb) * 16, 1u);
      }
      acquire_all();
      task_out(p, smem, s, cb);
    }
  }
}

__global__ __launch_bounds__(256, 1) void k_main(Params p) {
  extern __shared__ __attribute__((aligned(16))) char smem[];
  if (blockIdx.x < NH) recurrence(p, smem);
  else worker(p, smem);
}

extern "C" void kernel_launch(void* const* d_in, const int* in_sizes, int n_in, void* d_out, int out_size, void* d_ws,
                              size_t ws_size, hipStream_t stream) {
  Params p;
  p.x = (const u16*)d_in[0];
  p.mem0 = (const u16*)d_in[1];
  p.Wq = (const u16*)d_in[2];
  p.Wk = (const u16*)d_in[3];
  p.Wv = (const u16*)d_in[4];
  p.Wo = (const u16*)d_in[5];
  p.w1 = (const u16*)d_in[8];
  p.b1 = (const u16*)d_in[9];
  p.w2 = (const u16*)d_in[10];
  p.b2 = (const u16*)d_in[11];
  p.out = (u16*)d_out;
  char* ws = (char*)d_ws;
  size_t o = 0;
  auto take = [&](size_t bytes) { char* q = ws + o; o += (bytes + 255) & ~(size_t)255; return q; };
  p.fl = (unsigned*)take(F_WORDS * 4);
  p.P2 = (float*)take((size_t)NSEG * 4096 * 4);
  p.P1 = (float*)take((size_t)NSEG * 32 * 32 * 128 * 4);
  const size_t zbytes = o;
  p.xn = (u16*)take((size_t)S_ * DM * 2);
  p.y = (u16*)take((size_t)S_ * DM * 2);
  p.qkv = (u16*)take((size_t)S_ * NQKV * 2);
  p.h1 = (u16*)take((size_t)S_ * FF * 2);
  p.g = (u16*)take((size_t)S_ * DM * 2);
  p.kvm = (u16*)take((size_t)NSEG * MM * 8192 * 2);
  p.ox = (u16*)take((size_t)S_ * 4096 * 2);
  p.stamps = (unsigned long long*)(ws + 200 * 1024 * 1024);

  k_prep<<<512, 256, 0, stream>>>(p.x, p.xn, p.y, (float*)ws, (int)(zbytes / 16));
  hipFuncSetAttribute((const void*)k_main, hipFuncAttributeMaxDynamicSharedMemorySize, 163840);
  k_main<<<256, 256, 163840, stream>>>(p);
}
```

```cpp
#include <hip/hip_runtime.h>
#include <hip/hip_bf16.h>
#include <stdint.h>

typedef __attribute__((ext_vector_type(8))) __bf16 bf16x8;
typedef __attribute__((ext_vector_type(4))) __bf16 bf16x4;
typedef __attribute__((ext_vector_type(4))) float f32x4;
typedef __attribute__((ext_vector_type(4))) short s16x4;
typedef __attribute__((ext_vector_type(4))) unsigned u32x4;
typedef __attribute__((ext_vector_type(2))) unsigned u32x2;
typedef unsigned short u16;
#define LAS __attribute__((address_space(3)))

static constexpr int S_ = 2048, DM = 512, NH = 32, DH = 128, MM = 8, SEG = 32, NSEG = 64, FF = 1376;
static constexpr int NQKV = 3 * NH * DH;

__device__ __forceinline__ float bf2f(u16 v) { return __uint_as_float(((unsigned)v) << 16); }
__device__ __forceinline__ u16 f2bf(float f) {
  __bf16 b = (__bf16)f;
  return __builtin_bit_cast(u16, b);
}
__device__ __forceinline__ float rbf(float f) { return bf2f(f2bf(f)); }
typedef __attribute__((ext_vector_type(2))) float f32x2;
typedef __attribute__((ext_vector_type(2))) __bf16 bf16x2;
__device__ __forceinline__ unsigned pack2(float a, float b) {
  bf16x2 r = __builtin_convertvector((f32x2){a, b}, bf16x2);
  return __builtin_bit_cast(unsigned, r);
}

__global__ __launch_bounds__(256) void k_prep(const u16* __restrict__ x, u16* __restrict__ xn, u16* __restrict__ y,
                                              float* __restrict__ zb, int zn4) {
  int tid = threadIdx.x, w = tid >> 6, l = tid & 63;
  int row = blockIdx.x * 4 + w;
  {
    int gt = blockIdx.x * 256 + tid;
    for (int i = gt; i < zn4; i += 512 * 256) ((f32x4*)zb)[i] = (f32x4){0, 0, 0, 0};
  }
  u32x4 raw = *(const u32x4*)(x + (size_t)row * DM + l * 8);
  float v[8];
#pragma unroll
  for (int i = 0; i < 4; ++i) { v[2 * i] = bf2f(raw[i] & 0xffff); v[2 * i + 1] = bf2f(raw[i] >> 16); }
#pragma unroll
  for (int pass = 0; pass < 2; ++pass) {
    float s = 0.f;
#pragma unroll
    for (int i = 0; i < 8; ++i) s += v[i];
#pragma unroll
    for (int m = 32; m >= 1; m >>= 1) s += __shfl_xor(s, m);
    float mean = s * (1.f / DM);
    float q = 0.f;
#pragma unroll
    for (int i = 0; i < 8; ++i) { float d = v[i] - mean; q += d * d; }
#pragma unroll
    for (int m = 32; m >= 1; m >>= 1) q += __shfl_xor(q, m);
    float var = rbf(q * (1.f / DM));
    float mb = rbf(mean);
    float den = rbf(sqrtf(rbf(var + 1e-5f)));
#pragma unroll
    for (int i = 0; i < 8; ++i) v[i] = rbf(rbf(v[i] - mb) / den);
    u32x4 o;
#pragma unroll
    for (int i = 0; i < 4; ++i) o[i] = pack2(v[2 * i], v[2 * i + 1]);
    *(u32x4*)((pass == 0 ? xn : y) + (size_t)row * DM + l * 8) = o;
  }
}

struct Params {
  const u16 *x, *mem0, *Wq, *Wk, *Wv, *Wo, *w1, *b1, *w2, *b2;
  u16 *out, *xn, *y, *qkv, *h1, *g, *kvm, *ox;
  float *P1, *P2;
  unsigned* fl;
  unsigned long long* stamps;
};
#define RLX_AGENT __ATOMIC_RELAXED, __HIP_MEMORY_SCOPE_AGENT
static constexpr int F_TILE = 0;
static constexpr int F_QKV = 1024;
static constexpr int F_H = 1280;
static constexpr int F_G = 1536;
static constexpr int F_M = 2048;
static constexpr int F_OX = 3072;
static constexpr int F_OXB = 4096;
static constexpr int F_WORDS = 4096 + 64 * 32 * 16;
static constexpr unsigned O_FL = 0;
static constexpr unsigned O_P2 = F_WORDS * 4;
static constexpr unsigned O_P1 = O_P2 + NSEG * 4096 * 4;
static constexpr unsigned O_ZEND = O_P1 + NSEG * 32 * 32 * 128 * 4;
static constexpr unsigned O_XN = O_ZEND;
static constexpr unsigned O_Y = O_XN + S_ * DM * 2;
static constexpr unsigned O_QKV = O_Y + S_ * DM * 2;
static constexpr unsigned O_H1 = O_QKV + S_ * NQKV * 2;
static constexpr unsigned O_G = O_H1 + S_ * FF * 2;
static constexpr unsigned O_KVM = O_G + S_ * DM * 2;
static constexpr unsigned O_END = O_KVM + NSEG * MM * 8192 * 2;

__device__ __forceinline__ void st8_sc1(u16* p, u32x2 v) {
  __hip_atomic_store((unsigned long long*)p, (unsigned long long)v[0] | ((unsigned long long)v[1] << 32), RLX_AGENT);
}
__device__ __forceinline__ void publish(unsigned* c1, unsigned* c2) {
  asm volatile("s_waitcnt vmcnt(0)" ::: "memory");
  __syncthreads();
  if (threadIdx.x == 0) {
    __hip_atomic_fetch_add(c1, 1u, RLX_AGENT);
    if (c2) __hip_atomic_fetch_add(c2, 1u, RLX_AGENT);
  }
}
template <int SLP = 4>
__device__ __forceinline__ void wait1(unsigned* c, unsigned target) {
  unsigned spins = 0;
  while (__hip_atomic_load(c, RLX_AGENT) < target) {
    __builtin_amdgcn_s_sleep(SLP);
    if (++spins > (1u << 22)) break;
  }
}
__device__ __forceinline__ void acquire_all() {
  if (threadIdx.x == 0) {
    __builtin_amdgcn_fence(__ATOMIC_ACQUIRE, "agent");
    asm volatile("s_waitcnt vmcnt(0)" ::: "memory");
  }
  __syncthreads();
}

struct EpiStore {
  u16* C; int ldc; int pad_;
  __device__ __forceinline__ void operator()(int m, int n, f32x4 a) const {
    u32x2 o; o[0] = pack2(a[0], a[1]); o[1] = pack2(a[2], a[3]);
    st8_sc1(C + (size_t)m * ldc + n, o);
  }
};
struct EpiGelu {
  u16* C; int ldc; int pad_; const u16* b;
  __device__ __forceinline__ void operator()(int m, int n, f32x4 a) const {
    float r[4];
#pragma unroll
    for (int i = 0; i < 4; ++i) {
      float v = rbf(rbf(a[i]) + bf2f(b[n + i]));
      float u = 0.7978845608028654f * (v + 0.044715f * v * v * v);
      r[i] = 0.5f * v * (1.f + tanhf(u));
    }
    u32x2 o; o[0] = pack2(r[0], r[1]); o[1] = pack2(r[2], r[3]);
    st8_sc1(C + (size_t)m * ldc + n, o);
  }
};
struct EpiSilu {
  u16* C; int ldc; int pad_; const u16* b;
  __device__ __forceinline__ void operator()(int m, int n, f32x4 a) const {
    float r[4];
#pragma unroll
    for (int i = 0; i < 4; ++i) {
      float v = rbf(rbf(a[i]) + bf2f(b[n + i]));
      r[i] = v / (1.f + __expf(-v));
    }
    u32x2 o; o[0] = pack2(r[0], r[1]); o[1] = pack2(r[2], r[3]);
    st8_sc1(C + (size_t)m * ldc + n, o);
  }
};

template <int BM, int BN, int BK>
struct GemmCfg {
  static constexpr int ASTR = BK * 2 + 16;
  static constexpr int BSTR = BN * 2 + 32;
  static constexpr int ABYTES = BM * ASTR, BBYTES = BK * BSTR;
  static constexpr int LDS = 2 * (ABYTES + BBYTES);
};

template <int BM, int BN, int BK, class Epi>
__device__ __forceinline__ void gemm_tile(char* smem, const u16* __restrict__ A, int lda, const u16* __restrict__ B, int ldb,
                                          int M, int N, int K, int bm, int bn, const Epi& epi) {
  using C = GemmCfg<BM, BN, BK>;
  constexpr int WM = BM / 2, WN = BN / 2, TM = WM / 16, TN = WN / 16;
  constexpr int ACH = BM * BK / 8 / 256;
  constexpr int BCH = BK * BN / 8 / 256;
  static_assert(ACH >= 1 && BCH >= 1, "tile too small");
  const int tid = threadIdx.x, w = tid >> 6, l = tid & 63, g = l >> 4, c16 = l & 15;
  const int wm = w >> 1, wn = w & 1;
  f32x4 acc[TM][TN];
#pragma unroll
  for (int i = 0; i < TM; ++i)
#pragma unroll
    for (int j = 0; j < TN; ++j) acc[i][j] = (f32x4){0, 0, 0, 0};
  u32x4 ra[ACH], rb[BCH];
  auto gload = [&](int k0) {
#pragma unroll
    for (int i = 0; i < ACH; ++i) {
      int ch = tid + i * 256, r = ch / (BK / 8), c = ch % (BK / 8);
      int k = k0 + c * 8;
      ra[i] = (k < K) ? *(const u32x4*)(A + (size_t)(bm + r) * lda + k) : (u32x4){0, 0, 0, 0};
    }
#pragma unroll
    for (int i = 0; i < BCH; ++i) {
      int ch = tid + i * 256, r = ch / (BN / 8), c = ch % (BN / 8);
      int k = k0 + r, n = bn + c * 8;
      rb[i] = (k < K && n < N) ? *(const u32x4*)(B + (size_t)k * ldb + n) : (u32x4){0, 0, 0, 0};
    }
  };
  auto lstore = [&](int buf) {
    char* sa = smem + buf * (C::ABYTES + C::BBYTES);
    char* sb = sa + C::ABYTES;
#pragma unroll
    for (int i = 0; i < ACH; ++i) {
      int ch = tid + i * 256, r = ch / (BK / 8), c = ch % (BK / 8);
      *(u32x4*)(sa + r * C::ASTR + c * 16) = ra[i];
    }
#pragma unroll
    for (int i = 0; i < BCH; ++i) {
      int ch = tid + i * 256, r = ch / (BN / 8), c = ch % (BN / 8);
      *(u32x4*)(sb + r * C::BSTR + ((c ^ (((r >> 3) & 1) * (BN / 16))) * 16)) = rb[i];
    }
  };
  const int nk = (K + BK - 1) / BK;
  gload(0);
  lstore(0);
  __syncthreads();
  for (int kt = 0; kt < nk; ++kt) {
    int buf = kt & 1;
    if (kt + 1 < nk) gload((kt + 1) * BK);
    const char* sa = smem + buf * (C::ABYTES + C::BBYTES);
    const char* sb = sa + C::ABYTES;
#pragma unroll
    for (int ks = 0; ks < BK / 32; ++ks) {
      bf16x8 af[TM], bfr[TN];
#pragma unroll
      for (int i = 0; i < TM; ++i)
        af[i] = *(const bf16x8*)(sa + (wm * WM + i * 16 + c16) * C::ASTR + (ks * 32 + g * 8) * 2);
#pragma unroll
      for (int j = 0; j < TN; ++j) {
        int q = c16 >> 2, p = c16 & 3;
        int col = wn * WN + j * 16 + p * 4;
        s16x4 lo, hi;
        {
          int r = ks * 32 + g * 8 + q;
          lo = __builtin_amdgcn_ds_read_tr16_b64_v4i16((LAS s16x4*)(sb + r * C::BSTR + ((col * 2) ^ (((r >> 3) & 1) * BN))));
          r += 4;
          hi = __builtin_amdgcn_ds_read_tr16_b64_v4i16((LAS s16x4*)(sb + r * C::BSTR + ((col * 2) ^ (((r >> 3) & 1) * BN))));
        }
        typedef __attribute__((ext_vector_type(8))) short s16x8;
        s16x8 t = __builtin_shufflevector(lo, hi, 0, 1, 2, 3, 4, 5, 6, 7);
        bfr[j] = __builtin_bit_cast(bf16x8, t);
      }
#pragma unroll
      for (int i = 0; i < TM; ++i)
#pragma unroll
        for (int j = 0; j < TN; ++j) acc[i][j] = __builtin_amdgcn_mfma_f32_16x16x32_bf16(bfr[j], af[i], acc[i][j], 0, 0, 0);
    }
    if (kt + 1 < nk) lstore(buf ^ 1);
    __syncthreads();
  }
#pragma unroll
  for (int i = 0; i < TM; ++i)
#pragma unroll
    for (int j = 0; j < TN; ++j) {
      int m = bm + wm * WM + i * 16 + c16, n = bn + wn * WN + j * 16 + g * 4;
      if (n < N) epi(m, n, acc[i][j]);
    }
}

__device__ __forceinline__ unsigned swz(unsigned row, unsigned ch) {
  return 256u * row + 16u * (ch ^ (((row & 3) << 2) | ((row >> 2) & 3)));
}

static constexpr int L_WO = 0;
static constexpr int L_V = 131072;
static constexpr int L_K = L_V + 40 * 256;
static constexpr int L_Q = L_K + 40 * 256;
static constexpr int L_MEM = L_Q + 8 * 256;
static constexpr int L_END = L_MEM + 8704;
static_assert(L_END <= 163840, "lds");

#define FS(i)
#define STAMP(i)

__device__ __forceinline__ void recurrence(const Params& p, char* smem) {
  const u16* __restrict__ Wq = p.Wq; const u16* __restrict__ Wk = p.Wk; const u16* __restrict__ Wv = p.Wv;
  const u16* __restrict__ Wo = p.Wo; const u16* __restrict__ mem0 = p.mem0;
  unsigned long long* stamps = p.stamps;
  const int h = blockIdx.x;
  const __amdgpu_buffer_rsrc_t wr = __builtin_amdgcn_make_buffer_rsrc(p.fl, 0, O_END, 0x00020000);
  unsigned* const fl = p.fl;
  typedef __attribute__((ext_vector_type(8))) short s16x8;

  bf16x8 wf[3][2][16];
  {
    const int tid = threadIdx.x, w = tid >> 6, l = tid & 63, g = l >> 4, c16 = l & 15;
    const int q4 = c16 >> 2, p4 = c16 & 3;
#pragma unroll
    for (int m = 0; m < 3; ++m) {
      const u16* W = m == 0 ? Wq : (m == 1 ? Wk : Wv);
      for (int i0 = 0; i0 < 32; i0 += 8) {
        u32x4 v[8];
#pragma unroll
        for (int i = 0; i < 8; ++i) {
          int ch = tid + (i0 + i) * 256, r = ch >> 4, c = ch & 15;
          v[i] = *(const u32x4*)(W + (size_t)r * 4096 + h * 128 + c * 8);
        }
#pragma unroll
        for (int i = 0; i < 8; ++i) {
          int ch = tid + (i0 + i) * 256, r = ch >> 4, c = ch & 15;
          *(u32x4*)(smem + swz(r, c)) = v[i];
        }
      }
      __syncthreads();
#pragma unroll
      for (int c = 0; c < 2; ++c)
#pragma unroll
        for (int t = 0; t < 16; ++t) {
          int ct = 2 * w + c;
          int r0 = 32 * t + 8 * g + q4;
          s16x4 lo = __builtin_amdgcn_ds_read_tr16_b64_v4i16((LAS s16x4*)(smem + swz(r0, 2 * ct + (p4 >> 1)) + 8 * (p4 & 1)));
          s16x4 hi = __builtin_amdgcn_ds_read_tr16_b64_v4i16((LAS s16x4*)(smem + swz(r0 + 4, 2 * ct + (p4 >> 1)) + 8 * (p4 & 1)));
          s16x8 tt = __builtin_shufflevector(lo, hi, 0, 1, 2, 3, 4, 5, 6, 7);
          wf[m][c][t] = __builtin_bit_cast(bf16x8, tt);
        }
      __syncthreads();
    }
    for (int t = 0; t < 4; ++t) {
      char* st = smem + 131072;
      u32x4 v[8];
#pragma unroll
      for (int i = 0; i < 8; ++i) {
        int ch = tid + i * 256, r = ch >> 6, c = ch & 63;
        v[i] = *(const u32x4*)(Wo + (size_t)(h * 128 + t * 32 + r) * 512 + c * 8);
      }
#pragma unroll
      for (int i = 0; i < 8; ++i) {
        int ch = tid + i * 256, r = ch >> 6, c = ch & 63;
        *(u32x4*)(st + (c >> 4) * 8192 + swz(r, c & 15)) = v[i];
      }
      __syncthreads();
#pragma unroll
      for (int j = 0; j < 8; ++j) {
        int r0 = 8 * g + q4;
        s16x4 lo = __builtin_amdgcn_ds_read_tr16_b64_v4i16((LAS s16x4*)(st + w * 8192 + swz(r0, 2 * j + (p4 >> 1)) + 8 * (p4 & 1)));
        s16x4 hi = __builtin_amdgcn_ds_read_tr16_b64_v4i16((LAS s16x4*)(st + w * 8192 + swz(r0 + 4, 2 * j + (p4 >> 1)) + 8 * (p4 & 1)));
        s16x8 tt = __builtin_shufflevector(lo, hi, 0, 1, 2, 3, 4, 5, 6, 7);
        *(s16x8*)(smem + L_WO + ((w * 8 + j) * 4 + t) * 1024 + l * 16) = tt;
      }
      __syncthreads();
    }
    if (l == 0) { wait1(fl + F_TILE + h, 1u); wait1(fl + F_TILE + 32 + h, 1u); }
    asm volatile("" ::: "memory");
#pragma unroll
    for (int i = 0; i < 2; ++i) {
      int ch = tid + i * 256, r = ch >> 4, c = ch & 15;
      unsigned off = (unsigned)(r * NQKV + 4096 + h * 128 + c * 8) * 2;
      *(u32x4*)(smem + L_K + swz(8 + r, c)) = __builtin_amdgcn_raw_buffer_load_b128(wr, O_QKV + off, 0, 16);
      *(u32x4*)(smem + L_V + swz(8 + r, c)) = __builtin_amdgcn_raw_buffer_load_b128(wr, O_QKV + off + 8192, 0, 16);
    }
  }

  const float scale = 0.08838834764831845f;
  u32x4 pk[2], pv[2];

  for (int s = 0; s < NSEG; ++s) {
    int tid = threadIdx.x;
    asm volatile("" : "+v"(tid));
    const int w = tid >> 6, l = tid & 63, g = l >> 4, c16 = l & 15;
    const int q4 = c16 >> 2, p4 = c16 & 3;
    unsigned spins = 0;
    STAMP(0)
    if (s == 0) {
#pragma unroll
      for (int i = 0; i < 2; ++i) {
        int ch = tid + i * 256, r = ch >> 6, c = ch & 63;
        u32x4 v = *(const u32x4*)(mem0 + r * 512 + c * 8);
        *(u32x4*)(smem + L_MEM + r * 1088 + c * 16) = v;
      }
    } else {
      u32x4 a[4];
      while (true) {
        unsigned ok = 1u;
        asm volatile("" ::: "memory");
#pragma unroll
        for (int i = 0; i < 4; ++i) a[i] = __builtin_amdgcn_raw_buffer_load_b128(wr, O_P2 + (unsigned)(s * 4096) * 4 + (i * 256 + tid) * 16, 0, 16);
#pragma unroll
        for (int i = 0; i < 4; ++i) ok &= a[i][0] & a[i][1] & a[i][2] & a[i][3];
        if (((s + 1) & 3) == 0 && s + 1 < NSEG) {
          const unsigned fo = O_FL + (unsigned)(F_TILE + ((s + 1) >> 2) * 64 + h) * 4;
          ok &= __builtin_amdgcn_raw_buffer_load_b32(wr, fo, 0, 16) & __builtin_amdgcn_raw_buffer_load_b32(wr, fo + 128, 0, 16);
        }
        if (__all(ok & 1u) || ++spins > (1u << 20)) break;
      }
#pragma unroll
      for (int i = 0; i < 4; ++i) {
        int ch = i * 256 + tid, r = ch >> 5, qy = (ch >> 2) & 7, qd = ch & 3;
        u32x2 v; v[0] = pack2(__uint_as_float(a[i][0]), __uint_as_float(a[i][1])); v[1] = pack2(__uint_as_float(a[i][2]), __uint_as_float(a[i][3]));
        *(u32x2*)(smem + L_MEM + qy * 1088 + (16 * r + 4 * qd) * 2) = v;
      }
    }
    FS(0)
    __syncthreads();
    FS(1)
    if (s > 0 && threadIdx.x == 0) __hip_atomic_fetch_add(fl + F_M + (s - 1) * 16, 1u, RLX_AGENT);
    STAMP(1)
    f32x4 acc[3][2];
#pragma unroll
    for (int m = 0; m < 3; ++m)
#pragma unroll
      for (int c = 0; c < 2; ++c) acc[m][c] = (f32x4){0, 0, 0, 0};
    {
      const char* mb = smem + L_MEM + (c16 & 7) * 1088 + g * 16;
      bf16x8 mf[16];
#pragma unroll
      for (int t = 0; t < 3; ++t) mf[t] = *(const bf16x8*)(mb + t * 64);
#pragma unroll
      for (int t = 0; t < 16; ++t) {
        if (t + 3 < 16) mf[t + 3] = *(const bf16x8*)(mb + (t + 3) * 64);
#pragma unroll
        for (int m = 0; m < 3; ++m)
#pragma unroll
          for (int c = 0; c < 2; ++c) acc[m][c] = __builtin_amdgcn_mfma_f32_16x16x32_bf16(wf[m][c][t], mf[t], acc[m][c], 0, 0, 0);
        __builtin_amdgcn_sched_barrier(0);
      }
    }
    FS(2)
    if (c16 < 8) {
      const unsigned sw = (((c16 & 3) << 2) | ((c16 >> 2) & 3));
#pragma unroll
      for (int c = 0; c < 2; ++c) {
        const int chunk = 4 * w + 2 * c + (g >> 1);
        const unsigned off = 256u * c16 + 16u * (chunk ^ sw) + (g & 1) * 8;
        u32x2 vq, vk, vv;
        vq[0] = pack2(acc[0][c][0], acc[0][c][1]); vq[1] = pack2(acc[0][c][2], acc[0][c][3]);
        vk[0] = pack2(acc[1][c][0], acc[1][c][1]); vk[1] = pack2(acc[1][c][2], acc[1][c][3]);
        vv[0] = pack2(acc[2][c][0], acc[2][c][1]); vv[1] = pack2(acc[2][c][2], acc[2][c][3]);
        *(u32x2*)(smem + L_Q + off) = vq;
        *(u32x2*)(smem + L_K + off) = vk;
        *(u32x2*)(smem + L_V + off) = vv;
      }
    }
    FS(3)
    __syncthreads();
    FS(4)
    STAMP(2)
    f32x4 sc[3];
    s16x4 va[2][3];
    {
      bf16x8 qf[4], kf[3][4];
#pragma unroll
      for (int t = 0; t < 4; ++t) {
        qf[t] = *(const bf16x8*)(smem + L_Q + swz(c16 & 7, 4 * t + g));
        kf[0][t] = *(const bf16x8*)(smem + L_K + swz(c16, 4 * t + g));
      }
#pragma unroll
      for (int t = 0; t < 4; ++t) kf[1][t] = *(const bf16x8*)(smem + L_K + swz(16 + c16, 4 * t + g));
      __builtin_amdgcn_sched_barrier(0);
      sc[0] = (f32x4){0, 0, 0, 0};
#pragma unroll
      for (int t = 0; t < 4; ++t) sc[0] = __builtin_amdgcn_mfma_f32_16x16x32_bf16(kf[0][t], qf[t], sc[0], 0, 0, 0);
      __builtin_amdgcn_sched_barrier(0);
#pragma unroll
      for (int t = 0; t < 4; ++t) kf[2][t] = *(const bf16x8*)(smem + L_K + swz(32 + c16, 4 * t + g));
      sc[1] = (f32x4){0, 0, 0, 0};
#pragma unroll
      for (int t = 0; t < 4; ++t) sc[1] = __builtin_amdgcn_mfma_f32_16x16x32_bf16(kf[1][t], qf[t], sc[1], 0, 0, 0);
      __builtin_amdgcn_sched_barrier(0);
#pragma unroll
      for (int c = 0; c < 2; ++c) {
        int ch = 2 * (2 * w + c) + (p4 >> 1);
#pragma unroll
        for (int kt = 0; kt < 3; ++kt)
          va[c][kt] = __builtin_amdgcn_ds_read_tr16_b64_v4i16((LAS s16x4*)(smem + L_V + swz(16 * kt + 4 * g + q4, ch) + 8 * (p4 & 1)));
      }
      sc[2] = (f32x4){0, 0, 0, 0};
#pragma unroll
      for (int t = 0; t < 4; ++t) sc[2] = __builtin_amdgcn_mfma_f32_16x16x32_bf16(kf[2][t], qf[t], sc[2], 0, 0, 0);
    }
    __builtin_amdgcn_sched_barrier(0);
    if (s + 1 < NSEG) {
#pragma unroll
      for (int i = 0; i < 2; ++i) {
        int ch = tid + i * 256, r = ch >> 4, c = ch & 15;
        unsigned off = (unsigned)(((s + 1) * SEG + r) * NQKV + 4096 + h * 128 + c * 8) * 2;
        pk[i] = __builtin_amdgcn_raw_buffer_load_b128(wr, O_QKV + off, 0, 16);
        pv[i] = __builtin_amdgcn_raw_buffer_load_b128(wr, O_QKV + off + 8192, 0, 16);
      }
    }
    FS(5)
    bf16x8 pf0, pf1;
    {
      const bool hi = c16 >= 8;
      float xv[6];
#pragma unroll
      for (int r = 0; r < 4; ++r) xv[r] = hi ? sc[1][r] : sc[0][r];
      xv[4] = hi ? sc[2][2] : sc[2][0];
      xv[5] = hi ? sc[2][3] : sc[2][1];
      float mx = -3.0e38f;
#pragma unroll
      for (int r = 0; r < 6; ++r) {
        float v = rbf(rbf(xv[r]) * scale);
        if (r >= 4 && g >= 2) v = -3.0e38f;
        xv[r] = v;
        mx = fmaxf(mx, v);
      }
      mx = fmaxf(mx, __uint_as_float(__builtin_amdgcn_update_dpp(0, __float_as_uint(mx), 0x128, 0xf, 0xf, false)));
      {
        u32x2 r = __builtin_amdgcn_permlane16_swap(__float_as_uint(mx), __float_as_uint(mx), false, false);
        mx = fmaxf(__uint_as_float(r[0]), __uint_as_float(r[1]));
        r = __builtin_amdgcn_permlane32_swap(__float_as_uint(mx), __float_as_uint(mx), false, false);
        mx = fmaxf(__uint_as_float(r[0]), __uint_as_float(r[1]));
      }
      const float mxl = mx * 1.4426950408889634f;
      float sum = 0.f;
#pragma unroll
      for (int r = 0; r < 6; ++r) {
        float e = __builtin_amdgcn_exp2f(__builtin_fmaf(xv[r], 1.4426950408889634f, -mxl));
        if (r >= 4 && g >= 2) e = 0.f;
        xv[r] = e;
        sum += e;
      }
      sum += __uint_as_float(__builtin_amdgcn_update_dpp(0, __float_as_uint(sum), 0x128, 0xf, 0xf, false));
      {
        u32x2 r = __builtin_amdgcn_permlane16_swap(__float_as_uint(sum), __float_as_uint(sum), false, false);
        sum = __uint_as_float(r[0]) + __uint_as_float(r[1]);
        r = __builtin_amdgcn_permlane32_swap(__float_as_uint(sum), __float_as_uint(sum), false, false);
        sum = __uint_as_float(r[0]) + __uint_as_float(r[1]);
      }
      const float inv = __builtin_amdgcn_rcpf(sum);
      unsigned k0 = pack2(xv[0] * inv, xv[1] * inv), k1 = pack2(xv[2] * inv, xv[3] * inv), k2 = pack2(xv[4] * inv, xv[5] * inv);
      unsigned o0 = __builtin_amdgcn_update_dpp(0, k0, 0x128, 0xf, 0xf, false);
      unsigned o1 = __builtin_amdgcn_update_dpp(0, k1, 0x128, 0xf, 0xf, false);
      unsigned o2 = __builtin_amdgcn_update_dpp(0, k2, 0x128, 0xf, 0xf, false);
      u32x4 pb0, pb1;
      pb0[0] = k0; pb0[1] = k1; pb0[2] = o0; pb0[3] = o1;
      pb1[0] = k2; pb1[1] = o2; pb1[2] = 0; pb1[3] = 0;
      pf0 = __builtin_bit_cast(bf16x8, pb0); pf1 = __builtin_bit_cast(bf16x8, pb1);
    }
    FS(6)
    {
      f32x4 o[2];
#pragma unroll
      for (int c = 0; c < 2; ++c) {
        s16x8 v0 = __builtin_shufflevector(va[c][0], va[c][1], 0, 1, 2, 3, 4, 5, 6, 7);
        o[c] = __builtin_amdgcn_mfma_f32_16x16x32_bf16(__builtin_bit_cast(bf16x8, v0), pf0, (f32x4){0, 0, 0, 0}, 0, 0, 0);
      }
#pragma unroll
      for (int c = 0; c < 2; ++c) {
        s16x8 v1 = __builtin_shufflevector(va[c][2], va[c][2], 0, 1, 2, 3, 4, 5, 6, 7);
        o[c] = __builtin_amdgcn_mfma_f32_16x16x32_bf16(__builtin_bit_cast(bf16x8, v1), pf1, o[c], 0, 0, 0);
      }
      if (c16 < 8) {
        const unsigned sw = (((c16 & 3) << 2) | ((c16 >> 2) & 3));
#pragma unroll
        for (int c = 0; c < 2; ++c) {
          const int chunk = 4 * w + 2 * c + (g >> 1);
          u32x2 v; v[0] = pack2(o[c][0], o[c][1]); v[1] = pack2(o[c][2], o[c][3]);
          *(u32x2*)(smem + L_MEM + 256u * c16 + 16u * (chunk ^ sw) + (g & 1) * 8) = v;
        }
      }
    }
    FS(7)
    __syncthreads();
    FS(8)
    STAMP(3)
    if (s + 1 < NSEG) {
      bf16x8 of[4];
#pragma unroll
      for (int t = 0; t < 4; ++t) of[t] = *(const bf16x8*)(smem + L_MEM + swz(c16 & 7, 4 * t + g));
      const char* wb = smem + L_WO + w * 32768 + l * 16;
      bf16x8 bw[32];
      constexpr int PD = 8;
      const unsigned base = O_P1 + (unsigned)((((s + 1) * 32 + 8 * w + (c16 >> 3)) * 32 + h) * 128 + (c16 & 7) * 16 + 4 * g) * 4;
#pragma unroll
      for (int u = 0; u < PD; ++u) bw[u] = *(const bf16x8*)(wb + u * 1024);
      f32x4 de, dod;
#pragma unroll
      for (int jp = 0; jp <= 4; ++jp) {
        f32x4 d0, d1;
        if (jp < 4) {
#pragma unroll
          for (int t = 0; t < 8; ++t) {
            const int u = jp * 8 + t;
            if (u + PD < 32) bw[u + PD] = *(const bf16x8*)(wb + (u + PD) * 1024);
            if (t == 0) d0 = __builtin_amdgcn_mfma_f32_16x16x32_bf16(bw[u], of[0], (f32x4){0, 0, 0, 0}, 0, 0, 0);
            else if (t < 4) d0 = __builtin_amdgcn_mfma_f32_16x16x32_bf16(bw[u], of[t], d0, 0, 0, 0);
            else if (t == 4) d1 = __builtin_amdgcn_mfma_f32_16x16x32_bf16(bw[u], of[0], (f32x4){0, 0, 0, 0}, 0, 0, 0);
            else d1 = __builtin_amdgcn_mfma_f32_16x16x32_bf16(bw[u], of[t - 4], d1, 0, 0, 0);
          }
        }
        if (jp > 0) {
          u32x4 v;
#pragma unroll
          for (int r = 0; r < 4; ++r)
            v[r] = 1u | (unsigned)__builtin_amdgcn_update_dpp(__float_as_uint(de[r]), __float_as_uint(dod[r]), 0x128, 0xf, 0xc, false);
          __builtin_amdgcn_raw_buffer_store_b128(v, wr, base + (jp - 1) * (2 * 32 * 128 * 4), 0, 16);
        }
        de = d0; dod = d1;
        __builtin_amdgcn_sched_barrier(0);
      }
      FS(9)
      FS(10)
      STAMP(4)
      {
        int r = tid >> 5, c = tid & 31;
        u32x4 v = *(const u32x4*)(smem + (c < 16 ? L_K : L_V) + swz(r, c & 15));
        __builtin_amdgcn_raw_buffer_store_b128(v, wr, O_KVM + (unsigned)((s * MM + r) * 8192 + (c >> 4) * 4096 + h * 128 + (c & 15) * 8) * 2, 0, 16);
#pragma unroll
        for (int i = 0; i < 2; ++i) {
          int ch = tid + i * 256, rr = ch >> 4, cc = ch & 15;
          *(u32x4*)(smem + L_K + swz(8 + rr, cc)) = pk[i];
          *(u32x4*)(smem + L_V + swz(8 + rr, cc)) = pv[i];
        }
      }
      f32x4 t4;
      while (true) {
        u32x4 a[4]; unsigned ok = 1u;
        asm volatile("" ::: "memory");
#pragma unroll
        for (int i = 0; i < 4; ++i)
          a[i] = __builtin_amdgcn_raw_buffer_load_b128(wr, O_P1 + (unsigned)((((s + 1) * 32 + h) * 32 + i * 8 + (l >> 3)) * 128 + 32 * w + (l & 7) * 4) * 4, 0, 16);
        t4 = (f32x4){0, 0, 0, 0};
#pragma unroll
        for (int i = 0; i < 4; ++i) {
          ok &= a[i][0] & a[i][1] & a[i][2] & a[i][3];
#pragma unroll
          for (int r = 0; r < 4; ++r) t4[r] += __uint_as_float(a[i][r]);
        }
        if (__all(ok & 1u) || ++spins > (1u << 20)) break;
      }
      FS(11)
      STAMP(5)
      u32x4 v;
#pragma unroll
      for (int r = 0; r < 4; ++r) {
        float x = t4[r];
        x += __uint_as_float(__builtin_amdgcn_update_dpp(0, __float_as_uint(x), 0x128  , 0xf, 0xf, false));
        u32x2 q = __builtin_amdgcn_permlane16_swap(__float_as_uint(x), __float_as_uint(x), false, false);
        x = __uint_as_float(q[0]) + __uint_as_float(q[1]);
        q = __builtin_amdgcn_permlane32_swap(__float_as_uint(x), __float_as_uint(x), false, false);
        x = __uint_as_float(q[0]) + __uint_as_float(q[1]);
        v[r] = __float_as_uint(x) | 1u;
      }
      if (l < 8) __builtin_amdgcn_raw_buffer_store_b128(v, wr, O_P2 + (unsigned)((s + 1) * 4096 + h * 128 + 32 * w + l * 4) * 4, 0, 16);
      FS(12)
      STAMP(6)
    } else {
      int r = tid >> 5, c = tid & 31;
      u32x4 v = *(const u32x4*)(smem + (c < 16 ? L_K : L_V) + swz(r, c & 15));
      __builtin_amdgcn_raw_buffer_store_b128(v, wr, O_KVM + (unsigned)((s * MM + r) * 8192 + (c >> 4) * 4096 + h * 128 + (c & 15) * 8) * 2, 0, 16);
      asm volatile("s_waitcnt vmcnt(0)" ::: "memory");
      __syncthreads();
      if (threadIdx.x == 0) __hip_atomic_fetch_add(fl + F_M + s * 16, 1u, RLX_AGENT);
    }
  }
}

__device__ __forceinline__ void task_attn(const Params& p, char* smem, int s, int hg) {
  const int tid = threadIdx.x, w = tid >> 6, l = tid & 63, g = l >> 4, c16 = l & 15;
  const int q4 = c16 >> 2, p4 = c16 & 3;
  typedef __attribute__((ext_vector_type(8))) short s16x8;
  const u16* __restrict__ qkv = p.qkv;
  const u16* __restrict__ kvm = p.kvm;
  const float scale = 0.08838834764831845f;
  char* sv = smem + w * 48 * 256;
  const int h = hg * 4 + w;
  u32x4 vst[12];
#pragma unroll
  for (int i = 0; i < 12; ++i) {
    int ch = l + i * 64, r = ch >> 4, c = ch & 15;
    u32x4 v = (u32x4){0, 0, 0, 0};
    if (r < 8) v = *(const u32x4*)(kvm + (size_t)(s * MM + r) * 8192 + 4096 + h * 128 + c * 8);
    else if (r < 40) v = *(const u32x4*)(qkv + (size_t)(s * SEG + r - 8) * NQKV + 8192 + h * 128 + c * 8);
    vst[i] = v;
  }
  bf16x8 qf[4][2], kf[4][3];
#pragma unroll
  for (int t = 0; t < 4; ++t) {
#pragma unroll
    for (int qt = 0; qt < 2; ++qt)
      qf[t][qt] = *(const bf16x8*)(qkv + (size_t)(s * SEG + 16 * qt + c16) * NQKV + h * 128 + 32 * t + 8 * g);
#pragma unroll
    for (int kt = 0; kt < 3; ++kt) {
      int key = 16 * kt + c16;
      const u16* src = key < 8 ? kvm + (size_t)(s * MM + key) * 8192 + h * 128
                               : qkv + (size_t)(s * SEG + (key < 40 ? key - 8 : 0)) * NQKV + 4096 + h * 128;
      kf[t][kt] = *(const bf16x8*)(src + 32 * t + 8 * g);
    }
  }
#pragma unroll
  for (int i = 0; i < 12; ++i) {
    int ch = l + i * 64, r = ch >> 4, c = ch & 15;
    *(u32x4*)(sv + swz(r, c)) = vst[i];
  }
  f32x4 sc[3][2];
#pragma unroll
  for (int kt = 0; kt < 3; ++kt)
#pragma unroll
    for (int qt = 0; qt < 2; ++qt) sc[kt][qt] = (f32x4){0, 0, 0, 0};
#pragma unroll
  for (int t = 0; t < 4; ++t)
#pragma unroll
    for (int kt = 0; kt < 3; ++kt)
#pragma unroll
      for (int qt = 0; qt < 2; ++qt) sc[kt][qt] = __builtin_amdgcn_mfma_f32_16x16x32_bf16(kf[t][kt], qf[t][qt], sc[kt][qt], 0, 0, 0);
  bf16x8 pf0[2], pf1[2];
#pragma unroll
  for (int qt = 0; qt < 2; ++qt) {
    int qi = 8 + 16 * qt + c16;
    float mx = -3.0e38f;
#pragma unroll
    for (int kt = 0; kt < 3; ++kt)
#pragma unroll
      for (int r = 0; r < 4; ++r) {
        int key = 16 * kt + 4 * g + r;
        float v = rbf(rbf(sc[kt][qt][r]) * scale);
        if (key > qi) v = -1e9f;
        if (key >= 40) v = -3.0e38f;
        sc[kt][qt][r] = v;
        mx = fmaxf(mx, v);
      }
    mx = fmaxf(mx, __shfl_xor(mx, 16));
    mx = fmaxf(mx, __shfl_xor(mx, 32));
    float sum = 0.f;
#pragma unroll
    for (int kt = 0; kt < 3; ++kt)
#pragma unroll
      for (int r = 0; r < 4; ++r) {
        int key = 16 * kt + 4 * g + r;
        float e = (key >= 40) ? 0.f : __expf(sc[kt][qt][r] - mx);
        sc[kt][qt][r] = e;
        sum += e;
      }
    sum += __shfl_xor(sum, 16);
    sum += __shfl_xor(sum, 32);
    float inv = 1.f / sum;
    u32x4 b0, b1;
    b0[0] = pack2(sc[0][qt][0] * inv, sc[0][qt][1] * inv); b0[1] = pack2(sc[0][qt][2] * inv, sc[0][qt][3] * inv);
    b0[2] = pack2(sc[1][qt][0] * inv, sc[1][qt][1] * inv); b0[3] = pack2(sc[1][qt][2] * inv, sc[1][qt][3] * inv);
    b1[0] = pack2(sc[2][qt][0] * inv, sc[2][qt][1] * inv); b1[1] = pack2(sc[2][qt][2] * inv, sc[2][qt][3] * inv);
    b1[2] = 0; b1[3] = 0;
    pf0[qt] = __builtin_bit_cast(bf16x8, b0);
    pf1[qt] = __builtin_bit_cast(bf16x8, b1);
  }
  __builtin_amdgcn_s_waitcnt(0xc07f);
  __builtin_amdgcn_wave_barrier();
#pragma unroll
  for (int c = 0; c < 8; ++c) {
    int ch = 2 * c + (p4 >> 1);
    s16x4 a0 = __builtin_amdgcn_ds_read_tr16_b64_v4i16((LAS s16x4*)(sv + swz(4 * g + q4, ch) + 8 * (p4 & 1)));
    s16x4 a1 = __builtin_amdgcn_ds_read_tr16_b64_v4i16((LAS s16x4*)(sv + swz(16 + 4 * g + q4, ch) + 8 * (p4 & 1)));
    s16x4 a2 = __builtin_amdgcn_ds_read_tr16_b64_v4i16((LAS s16x4*)(sv + swz(32 + 4 * g + q4, ch) + 8 * (p4 & 1)));
    s16x8 v0 = __builtin_shufflevector(a0, a1, 0, 1, 2, 3, 4, 5, 6, 7);
    s16x8 v1 = __builtin_shufflevector(a2, a2, 0, 1, 2, 3, 4, 5, 6, 7);
#pragma unroll
    for (int qt = 0; qt < 2; ++qt) {
      f32x4 o = (f32x4){0, 0, 0, 0};
      o = __builtin_amdgcn_mfma_f32_16x16x32_bf16(__builtin_bit_cast(bf16x8, v0), pf0[qt], o, 0, 0, 0);
      o = __builtin_amdgcn_mfma_f32_16x16x32_bf16(__builtin_bit_cast(bf16x8, v1), pf1[qt], o, 0, 0, 0);
      u32x2 v; v[0] = pack2(o[0], o[1]); v[1] = pack2(o[2], o[3]);
      st8_sc1(p.ox + (size_t)(s * SEG + 16 * qt + c16) * 4096 + h * 128 + 16 * c + 4 * g, v);
    }
  }
}

static constexpr int E_WO = 0;
static constexpr int E_RED = 131072;
__device__ __forceinline__ void load_wo_slice(const Params& p, char* smem, int cb) {
  const int tid = threadIdx.x, w = tid >> 6, l = tid & 63, g = l >> 4, c16 = l & 15;
  const int q4 = c16 >> 2, p4 = c16 & 3;
  typedef __attribute__((ext_vector_type(8))) short s16x8;
  char* st = smem + E_RED;
  for (int r8 = 0; r8 < 8; ++r8) {
    u32x4 v[4];
#pragma unroll
    for (int i = 0; i < 4; ++i) {
      int ch = tid + i * 256, r = ch >> 1, c = ch & 1;
      v[i] = *(const u32x4*)(p.Wo + (size_t)(r8 * 512 + r) * DM + cb * 16 + c * 8);
    }
#pragma unroll
    for (int i = 0; i < 4; ++i) {
      int ch = tid + i * 256, r = ch >> 1, c = ch & 1;
      *(u32x4*)(st + r * 32 + c * 16) = v[i];
    }
    __syncthreads();
#pragma unroll
    for (int t = 0; t < 4; ++t) {
      int ks = 4 * w + t;
      int r0 = 32 * ks + 8 * g + q4;
      s16x4 lo = __builtin_amdgcn_ds_read_tr16_b64_v4i16((LAS s16x4*)(st + r0 * 32 + p4 * 8));
      s16x4 hi = __builtin_amdgcn_ds_read_tr16_b64_v4i16((LAS s16x4*)(st + (r0 + 4) * 32 + p4 * 8));
      s16x8 tt = __builtin_shufflevector(lo, hi, 0, 1, 2, 3, 4, 5, 6, 7);
      *(s16x8*)(smem + E_WO + (r8 * 16 + ks) * 1024 + l * 16) = tt;
    }
    __syncthreads();
  }
}
__device__ __forceinline__ void task_out(const Params& p, char* smem, int s, int cb) {
  const int tid = threadIdx.x, w = tid >> 6, l = tid & 63, g = l >> 4, c16 = l & 15;
  f32x4 acc[2];
  acc[0] = (f32x4){0, 0, 0, 0}; acc[1] = (f32x4){0, 0, 0, 0};
  const u16* a0 = p.ox + (size_t)(s * SEG + c16) * 4096 + w * 1024 + 8 * g;
  const u16* a1 = a0 + 16 * 4096;
  const char* wb = smem + E_WO + w * 32768 + l * 16;
#pragma unroll 8
  for (int t = 0; t < 32; ++t) {
    bf16x8 x0 = *(const bf16x8*)(a0 + 32 * t), x1 = *(const bf16x8*)(a1 + 32 * t);
    bf16x8 wf = *(const bf16x8*)(wb + t * 1024);
    acc[0] = __builtin_amdgcn_mfma_f32_16x16x32_bf16(wf, x0, acc[0], 0, 0, 0);
    acc[1] = __builtin_amdgcn_mfma_f32_16x16x32_bf16(wf, x1, acc[1], 0, 0, 0);
  }
  *(f32x4*)(smem + E_RED + (w * 2 + 0) * 1024 + l * 16) = acc[0];
  *(f32x4*)(smem + E_RED + (w * 2 + 1) * 1024 + l * 16) = acc[1];
  __syncthreads();
  if (w < 2) {
    f32x4 a = *(f32x4*)(smem + E_RED + w * 1024 + l * 16);
#pragma unroll
    for (int i = 1; i < 4; ++i) a += *(f32x4*)(smem + E_RED + (i * 2 + w) * 1024 + l * 16);
    int m = s * SEG + 16 * w + c16, n = cb * 16 + 4 * g;
    u32x2 gg = *(const u32x2*)(p.g + (size_t)m * DM + n), xx = *(const u32x2*)(p.x + (size_t)m * DM + n);
    float gf[4] = {bf2f(gg[0] & 0xffff), bf2f(gg[0] >> 16), bf2f(gg[1] & 0xffff), bf2f(gg[1] >> 16)};
    float xf[4] = {bf2f(xx[0] & 0xffff), bf2f(xx[0] >> 16), bf2f(xx[1] & 0xffff), bf2f(xx[1] >> 16)};
    float r[4];
#pragma unroll
    for (int i = 0; i < 4; ++i) r[i] = xf[i] + rbf(rbf(a[i]) * gf[i]);
    u32x2 o; o[0] = pack2(r[0], r[1]); o[1] = pack2(r[2], r[3]);
    *(u32x2*)(p.out + (size_t)m * DM + n) = o;
  }
  __syncthreads();
}

__device__ __forceinline__ void worker(const Params& p, char* smem) {
  constexpr int NWK = 224;
  const int wid = blockIdx.x - NH;
  for (int t = wid; t < 1024; t += NWK) {
    int rb = t >> 6, ct = t & 63;
    const u16* B = ct < 32 ? p.Wk : p.Wv;
    EpiStore epi{p.qkv + 4096 + (ct >> 5) * 4096, NQKV, 0};
    gemm_tile<128, 128, 64, EpiStore>(smem, p.xn, DM, B, 4096, S_, 4096, DM, rb * 128, (ct & 31) * 128, epi);
    publish(p.fl + F_TILE + t, p.fl + F_QKV + rb * 16);
  }
  for (int t = (wid + 224 - (1024 % NWK)) % NWK; t < 176; t += NWK) {
    int rb = t / 11, ct = t % 11;
    EpiGelu epi{p.h1, FF, 0, p.b1};
    gemm_tile<128, 128, 64, EpiGelu>(smem, p.y, DM, p.w1, FF, S_, FF, DM, rb * 128, ct * 128, epi);
    publish(p.fl + F_H + rb * 16, nullptr);
  }
  for (int t = (wid + 48) % NWK; t < 512; t += NWK) {
    int rb = t >> 5, ct = t & 31;
    EpiStore epi{p.qkv, NQKV, 0};
    gemm_tile<128, 128, 64, EpiStore>(smem, p.xn, DM, p.Wq, 4096, S_, 4096, DM, rb * 128, ct * 128, epi);
    publish(p.fl + F_QKV + rb * 16, nullptr);
  }
  for (int t = (wid + 120) % NWK; t < 64; t += NWK) {
    int rb = t >> 2, ct = t & 3;
    if (threadIdx.x == 0) wait1<64>(p.fl + F_H + rb * 16, 11u);
    acquire_all();
    EpiSilu epi{p.g, DM, 0, p.b2};
    gemm_tile<128, 128, 64, EpiSilu>(smem, p.h1, FF, p.w2, DM, S_, DM, FF, rb * 128, ct * 128, epi);
    publish(p.fl + F_G + rb * 16, nullptr);
  }
  if (wid < 32) {
    for (int s = wid >> 3; s < NSEG; s += 4) {
      if (threadIdx.x == 0) {
        wait1<64>(p.fl + F_QKV + (s >> 2) * 16, 96u);
        wait1<64>(p.fl + F_M + s * 16, 32u);
      }
      acquire_all();
      task_attn(p, smem, s, wid & 7);
      asm volatile("s_waitcnt vmcnt(0)" ::: "memory");
      __syncthreads();
      if (threadIdx.x == 0) {
        unsigned old = __hip_atomic_fetch_add(p.fl + F_OX + s * 16, 1u, RLX_AGENT);
        if (old == 7u)
          for (int c = 0; c < 32; ++c) __hip_atomic_store(p.fl + F_OXB + (s * 32 + c) * 16, 1u, RLX_AGENT);
      }
    }
  } else {
    const int j = wid - 32, cb = j & 31, grp = j >> 5;
    load_wo_slice(p, smem, cb);
    for (int s = grp; s < NSEG; s += 6) {
      if (threadIdx.x == 0) {
        wait1<64>(p.fl + F_G + (s >> 2) * 16, 4u);
        wait1<64>(p.fl + F_OXB + (s * 32 + cb) * 16, 1u);
      }
      acquire_all();
      task_out(p, smem, s, cb);
    }
  }
}

__global__ __launch_bounds__(256, 1) void k_main(Params p) {
  extern __shared__ __attribute__((aligned(16))) char smem[];
  if (blockIdx.x < NH) recurrence(p, smem);
  else worker(p, smem);
}

extern "C" void kernel_launch(void* const* d_in, const int* in_sizes, int n_in, void* d_out, int out_size, void* d_ws,
                              size_t ws_size, hipStream_t stream) {
  Params p;
  p.x = (const u16*)d_in[0];
  p.mem0 = (const u16*)d_in[1];
  p.Wq = (const u16*)d_in[2];
  p.Wk = (const u16*)d_in[3];
  p.Wv = (const u16*)d_in[4];
  p.Wo = (const u16*)d_in[5];
  p.w1 = (const u16*)d_in[8];
  p.b1 = (const u16*)d_in[9];
  p.w2 = (const u16*)d_in[10];
  p.b2 = (const u16*)d_in[11];
  p.out = (u16*)d_out;
  char* ws = (char*)d_ws;
  size_t o = 0;
  auto take = [&](size_t bytes) { char* q = ws + o; o += (bytes + 255) & ~(size_t)255; return q; };
  p.fl = (unsigned*)take(F_WORDS * 4);
  p.P2 = (float*)take((size_t)NSEG * 4096 * 4);
  p.P1 = (float*)take((size_t)NSEG * 32 * 32 * 128 * 4);
  const size_t zbytes = o;
  p.xn = (u16*)take((size_t)S_ * DM * 2);
  p.y = (u16*)take((size_t)S_ * DM * 2);
  p.qkv = (u16*)take((size_t)S_ * NQKV * 2);
  p.h1 = (u16*)take((size_t)S_ * FF * 2);
  p.g = (u16*)take((size_t)S_ * DM * 2);
  p.kvm = (u16*)take((size_t)NSEG * MM * 8192 * 2);
  p.ox = (u16*)take((size_t)S_ * 4096 * 2);
  p.stamps = (unsigned long long*)(ws + 200 * 1024 * 1024);

  k_prep<<<512, 256, 0, stream>>>(p.x, p.xn, p.y, (float*)ws, (int)(zbytes / 16));
  hipFuncSetAttribute((const void*)k_main, hipFuncAttributeMaxDynamicSharedMemorySize, 163840);
  k_main<<<256, 256, 163840, stream>>>(p);
}
```

```cpp
#include <hip/hip_runtime.h>
#include <hip/hip_bf16.h>
#include <stdint.h>

typedef __attribute__((ext_vector_type(8))) __bf16 bf16x8;
typedef __attribute__((ext_vector_type(4))) __bf16 bf16x4;
typedef __attribute__((ext_vector_type(4))) float f32x4;
typedef __attribute__((ext_vector_type(4))) short s16x4;
typedef __attribute__((ext_vector_type(4))) unsigned u32x4;
typedef __attribute__((ext_vector_type(2))) unsigned u32x2;
typedef unsigned short u16;
#define LAS __attribute__((address_space(3)))

static constexpr int S_ = 2048, DM = 512, NH = 32, DH = 128, MM = 8, SEG = 32, NSEG = 64, FF = 1376;
static constexpr int NQKV = 3 * NH * DH;

__device__ __forceinline__ float bf2f(u16 v) { return __uint_as_float(((unsigned)v) << 16); }
__device__ __forceinline__ u16 f2bf(float f) {
  __bf16 b = (__bf16)f;
  return __builtin_bit_cast(u16, b);
}
__device__ __forceinline__ float rbf(float f) { return bf2f(f2bf(f)); }
typedef __attribute__((ext_vector_type(2))) float f32x2;
typedef __attribute__((ext_vector_type(2))) __bf16 bf16x2;
__device__ __forceinline__ unsigned pack2(float a, float b) {
  bf16x2 r = __builtin_convertvector((f32x2){a, b}, bf16x2);
  return __builtin_bit_cast(unsigned, r);
}

__global__ __launch_bounds__(256) void k_prep(const u16* __restrict__ x, u16* __restrict__ xn, u16* __restrict__ y,
                                              float* __restrict__ zb, int zn4) {
  int tid = threadIdx.x, w = tid >> 6, l = tid & 63;
  int row = blockIdx.x * 4 + w;
  {
    int gt = blockIdx.x * 256 + tid;
    for (int i = gt; i < zn4; i += 512 * 256) ((f32x4*)zb)[i] = (f32x4){0, 0, 0, 0};
  }
  u32x4 raw = *(const u32x4*)(x + (size_t)row * DM + l * 8);
  float v[8];
#pragma unroll
  for (int i = 0; i < 4; ++i) { v[2 * i] = bf2f(raw[i] & 0xffff); v[2 * i + 1] = bf2f(raw[i] >> 16); }
#pragma unroll
  for (int pass = 0; pass < 2; ++pass) {
    float s = 0.f;
#pragma unroll
    for (int i = 0; i < 8; ++i) s += v[i];
#pragma unroll
    for (int m = 32; m >= 1; m >>= 1) s += __shfl_xor(s, m);
    float mean = s * (1.f / DM);
    float q = 0.f;
#pragma unroll
    for (int i = 0; i < 8; ++i) { float d = v[i] - mean; q += d * d; }
#pragma unroll
    for (int m = 32; m >= 1; m >>= 1) q += __shfl_xor(q, m);
    float var = rbf(q * (1.f / DM));
    float mb = rbf(mean);
    float den = rbf(sqrtf(rbf(var + 1e-5f)));
#pragma unroll
    for (int i = 0; i < 8; ++i) v[i] = rbf(rbf(v[i] - mb) / den);
    u32x4 o;
#pragma unroll
    for (int i = 0; i < 4; ++i) o[i] = pack2(v[2 * i], v[2 * i + 1]);
    *(u32x4*)((pass == 0 ? xn : y) + (size_t)row * DM + l * 8) = o;
  }
}

struct Params {
  const u16 *x, *mem0, *Wq, *Wk, *Wv, *Wo, *w1, *b1, *w2, *b2;
  u16 *out, *xn, *y, *qkv, *h1, *g, *kvm, *ox;
  float *P1, *P2;
  unsigned* fl;
  unsigned long long* stamps;
};
#define RLX_AGENT __ATOMIC_RELAXED, __HIP_MEMORY_SCOPE_AGENT
static constexpr int F_TILE = 0;
static constexpr int F_QKV = 1024;
static constexpr int F_H = 1280;
static constexpr int F_G = 1536;
static constexpr int F_M = 2048;
static constexpr int F_OX = 3072;
static constexpr int F_OXB = 4096;
static constexpr int F_WORDS = 4096 + 64 * 32 * 16;
static constexpr unsigned O_FL = 0;
static constexpr unsigned O_P2 = F_WORDS * 4;
static constexpr unsigned O_P1 = O_P2 + NSEG * 4096 * 4;
static constexpr unsigned O_ZEND = O_P1 + NSEG * 32 * 32 * 128 * 4;
static constexpr unsigned O_XN = O_ZEND;
static constexpr unsigned O_Y = O_XN + S_ * DM * 2;
static constexpr unsigned O_QKV = O_Y + S_ * DM * 2;
static constexpr unsigned O_H1 = O_QKV + S_ * NQKV * 2;
static constexpr unsigned O_G = O_H1 + S_ * FF * 2;
static constexpr unsigned O_KVM = O_G + S_ * DM * 2;
static constexpr unsigned O_END = O_KVM + NSEG * MM * 8192 * 2;

__device__ __forceinline__ void st8_sc1(u16* p, u32x2 v) {
  __hip_atomic_store((unsigned long long*)p, (unsigned long long)v[0] | ((unsigned long long)v[1] << 32), RLX_AGENT);
}
__device__ __forceinline__ void publish(unsigned* c1, unsigned* c2) {
  asm volatile("s_waitcnt vmcnt(0)" ::: "memory");
  __syncthreads();
  if (threadIdx.x == 0) {
    __hip_atomic_fetch_add(c1, 1u, RLX_AGENT);
    if (c2) __hip_atomic_fetch_add(c2, 1u, RLX_AGENT);
  }
}
template <int SLP = 4>
__device__ __forceinline__ void wait1(unsigned* c, unsigned target) {
  unsigned spins = 0;
  while (__hip_atomic_load(c, RLX_AGENT) < target) {
    __builtin_amdgcn_s_sleep(SLP);
    if (++spins > (1u << 22)) break;
  }
}
__device__ __forceinline__ void acquire_all() {
  if (threadIdx.x == 0) {
    __builtin_amdgcn_fence(__ATOMIC_ACQUIRE, "agent");
    asm volatile("s_waitcnt vmcnt(0)" ::: "memory");
  }
  __syncthreads();
}

struct EpiStore {
  u16* C; int ldc; int pad_;
  __device__ __forceinline__ void operator()(int m, int n, f32x4 a) const {
    u32x2 o; o[0] = pack2(a[0], a[1]); o[1] = pack2(a[2], a[3]);
    st8_sc1(C + (size_t)m * ldc + n, o);
  }
};
struct EpiGelu {
  u16* C; int ldc; int pad_; const u16* b;
  __device__ __forceinline__ void operator()(int m, int n, f32x4 a) const {
    float r[4];
#pragma unroll
    for (int i = 0; i < 4; ++i) {
      float v = rbf(rbf(a[i]) + bf2f(b[n + i]));
      float u = 0.7978845608028654f * (v + 0.044715f * v * v * v);
      r[i] = 0.5f * v * (1.f + tanhf(u));
    }
    u32x2 o; o[0] = pack2(r[0], r[1]); o[1] = pack2(r[2], r[3]);
    st8_sc1(C + (size_t)m * ldc + n, o);
  }
};
struct EpiSilu {
  u16* C; int ldc; int pad_; const u16* b;
  __device__ __forceinline__ void operator()(int m, int n, f32x4 a) const {
    float r[4];
#pragma unroll
    for (int i = 0; i < 4; ++i) {
      float v = rbf(rbf(a[i]) + bf2f(b[n + i]));
      r[i] = v / (1.f + __expf(-v));
    }
    u32x2 o; o[0] = pack2(r[0], r[1]); o[1] = pack2(r[2], r[3]);
    st8_sc1(C + (size_t)m * ldc + n, o);
  }
};

template <int BM, int BN, int BK>
struct GemmCfg {
  static constexpr int ASTR = BK * 2 + 16;
  static constexpr int BSTR = BN * 2 + 32;
  static constexpr int ABYTES = BM * ASTR, BBYTES = BK * BSTR;
  static constexpr int LDS = 2 * (ABYTES + BBYTES);
};

template <int BM, int BN, int BK, class Epi>
__device__ __forceinline__ void gemm_tile(char* smem, const u16* __restrict__ A, int lda, const u16* __restrict__ B, int ldb,
                                          int M, int N, int K, int bm, int bn, const Epi& epi) {
  using C = GemmCfg<BM, BN, BK>;
  constexpr int WM = BM / 2, WN = BN / 2, TM = WM / 16, TN = WN / 16;
  constexpr int ACH = BM * BK / 8 / 256;
  constexpr int BCH = BK * BN / 8 / 256;
  static_assert(ACH >= 1 && BCH >= 1, "tile too small");
  const int tid = threadIdx.x, w = tid >> 6, l = tid & 63, g = l >> 4, c16 = l & 15;
  const int wm = w >> 1, wn = w & 1;
  f32x4 acc[TM][TN];
#pragma unroll
  for (int i = 0; i < TM; ++i)
#pragma unroll
    for (int j = 0; j < TN; ++j) acc[i][j] = (f32x4){0, 0, 0, 0};
  u32x4 ra[ACH], rb[BCH];
  auto gload = [&](int k0) {
#pragma unroll
    for (int i = 0; i < ACH; ++i) {
      int ch = tid + i * 256, r = ch / (BK / 8), c = ch % (BK / 8);
      int k = k0 + c * 8;
      ra[i] = (k < K) ? *(const u32x4*)(A + (size_t)(bm + r) * lda + k) : (u32x4){0, 0, 0, 0};
    }
#pragma unroll
    for (int i = 0; i < BCH; ++i) {
      int ch = tid + i * 256, r = ch / (BN / 8), c = ch % (BN / 8);
      int k = k0 + r, n = bn + c * 8;
      rb[i] = (k < K && n < N) ? *(const u32x4*)(B + (size_t)k * ldb + n) : (u32x4){0, 0, 0, 0};
    }
  };
  auto lstore = [&](int buf) {
    char* sa = smem + buf * (C::ABYTES + C::BBYTES);
    char* sb = sa + C::ABYTES;
#pragma unroll
    for (int i = 0; i < ACH; ++i) {
      int ch = tid + i * 256, r = ch / (BK / 8), c = ch % (BK / 8);
      *(u32x4*)(sa + r * C::ASTR + c * 16) = ra[i];
    }
#pragma unroll
    for (int i = 0; i < BCH; ++i) {
      int ch = tid + i * 256, r = ch / (BN / 8), c = ch % (BN / 8);
      *(u32x4*)(sb + r * C::BSTR + ((c ^ (((r >> 3) & 1) * (BN / 16))) * 16)) = rb[i];
    }
  };
  const int nk = (K + BK - 1) / BK;
  gload(0);
  lstore(0);
  __syncthreads();
  for (int kt = 0; kt < nk; ++kt) {
    int buf = kt & 1;
    if (kt + 1 < nk) gload((kt + 1) * BK);
    const char* sa = smem + buf * (C::ABYTES + C::BBYTES);
    const char* sb = sa + C::ABYTES;
#pragma unroll
    for (int ks = 0; ks < BK / 32; ++ks) {
      bf16x8 af[TM], bfr[TN];
#pragma unroll
      for (int i = 0; i < TM; ++i)
        af[i] = *(const bf16x8*)(sa + (wm * WM + i * 16 + c16) * C::ASTR + (ks * 32 + g * 8) * 2);
#pragma unroll
      for (int j = 0; j < TN; ++j) {
        int q = c16 >> 2, p = c16 & 3;
        int col = wn * WN + j * 16 + p * 4;
        s16x4 lo, hi;
        {
          int r = ks * 32 + g * 8 + q;
          lo = __builtin_amdgcn_ds_read_tr16_b64_v4i16((LAS s16x4*)(sb + r * C::BSTR + ((col * 2) ^ (((r >> 3) & 1) * BN))));
          r += 4;
          hi = __builtin_amdgcn_ds_read_tr16_b64_v4i16((LAS s16x4*)(sb + r * C::BSTR + ((col * 2) ^ (((r >> 3) & 1) * BN))));
        }
        typedef __attribute__((ext_vector_type(8))) short s16x8;
        s16x8 t = __builtin_shufflevector(lo, hi, 0, 1, 2, 3, 4, 5, 6, 7);
        bfr[j] = __builtin_bit_cast(bf16x8, t);
      }
#pragma unroll
      for (int i = 0; i < TM; ++i)
#pragma unroll
        for (int j = 0; j < TN; ++j) acc[i][j] = __builtin_amdgcn_mfma_f32_16x16x32_bf16(bfr[j], af[i], acc[i][j], 0, 0, 0);
    }
    if (kt + 1 < nk) lstore(buf ^ 1);
    __syncthreads();
  }
#pragma unroll
  for (int i = 0; i < TM; ++i)
#pragma unroll
    for (int j = 0; j < TN; ++j) {
      int m = bm + wm * WM + i * 16 + c16, n = bn + wn * WN + j * 16 + g * 4;
      if (n < N) epi(m, n, acc[i][j]);
    }
}

__device__ __forceinline__ unsigned swz(unsigned row, unsigned ch) {
  return 256u * row + 16u * (ch ^ (((row & 3) << 2) | ((row >> 2) & 3)));
}

static constexpr int L_WO = 0;
static constexpr int L_V = 131072;
static constexpr int L_K = L_V + 40 * 256;
static constexpr int L_Q = L_K + 40 * 256;
static constexpr int L_MEM = L_Q + 8 * 256;
static constexpr int L_END = L_MEM + 8704;
static_assert(L_END <= 163840, "lds");

#define FS(i)
#define STAMP(i)

__device__ __forceinline__ void recurrence(const Params& p, char* smem) {
  const u16* __restrict__ Wq = p.Wq; const u16* __restrict__ Wk = p.Wk; const u16* __restrict__ Wv = p.Wv;
  const u16* __restrict__ Wo = p.Wo; const u16* __restrict__ mem0 = p.mem0;
  unsigned long long* stamps = p.stamps;
  const int h = blockIdx.x;
  const __amdgpu_buffer_rsrc_t wr = __builtin_amdgcn_make_buffer_rsrc(p.fl, 0, O_END, 0x00020000);
  unsigned* const fl = p.fl;
  typedef __attribute__((ext_vector_type(8))) short s16x8;

  bf16x8 wf[3][2][16];
  {
    const int tid = threadIdx.x, w = tid >> 6, l = tid & 63, g = l >> 4, c16 = l & 15;
    const int q4 = c16 >> 2, p4 = c16 & 3;
#pragma unroll
    for (int m = 0; m < 3; ++m) {
      const u16* W = m == 0 ? Wq : (m == 1 ? Wk : Wv);
      for (int i0 = 0; i0 < 32; i0 += 8) {
        u32x4 v[8];
#pragma unroll
        for (int i = 0; i < 8; ++i) {
          int ch = tid + (i0 + i) * 256, r = ch >> 4, c = ch & 15;
          v[i] = *(const u32x4*)(W + (size_t)r * 4096 + h * 128 + c * 8);
        }
#pragma unroll
        for (int i = 0; i < 8; ++i) {
          int ch = tid + (i0 + i) * 256, r = ch >> 4, c = ch & 15;
          *(u32x4*)(smem + swz(r, c)) = v[i];
        }
      }
      __syncthreads();
#pragma unroll
      for (int c = 0; c < 2; ++c)
#pragma unroll
        for (int t = 0; t < 16; ++t) {
          int ct = 2 * w + c;
          int r0 = 32 * t + 8 * g + q4;
          s16x4 lo = __builtin_amdgcn_ds_read_tr16_b64_v4i16((LAS s16x4*)(smem + swz(r0, 2 * ct + (p4 >> 1)) + 8 * (p4 & 1)));
          s16x4 hi = __builtin_amdgcn_ds_read_tr16_b64_v4i16((LAS s16x4*)(smem + swz(r0 + 4, 2 * ct + (p4 >> 1)) + 8 * (p4 & 1)));
          s16x8 tt = __builtin_shufflevector(lo, hi, 0, 1, 2, 3, 4, 5, 6, 7);
          wf[m][c][t] = __builtin_bit_cast(bf16x8, tt);
        }
      __syncthreads();
    }
    for (int t = 0; t < 4; ++t) {
      char* st = smem + 131072;
      u32x4 v[8];
#pragma unroll
      for (int i = 0; i < 8; ++i) {
        int ch = tid + i * 256, r = ch >> 6, c = ch & 63;
        v[i] = *(const u32x4*)(Wo + (size_t)(h * 128 + t * 32 + r) * 512 + c * 8);
      }
#pragma unroll
      for (int i = 0; i < 8; ++i) {
        int ch = tid + i * 256, r = ch >> 6, c = ch & 63;
        *(u32x4*)(st + (c >> 4) * 8192 + swz(r, c & 15)) = v[i];
      }
      __syncthreads();
#pragma unroll
      for (int j = 0; j < 8; ++j) {
        int r0 = 8 * g + q4;
        s16x4 lo = __builtin_amdgcn_ds_read_tr16_b64_v4i16((LAS s16x4*)(st + w * 8192 + swz(r0, 2 * j + (p4 >> 1)) + 8 * (p4 & 1)));
        s16x4 hi = __builtin_amdgcn_ds_read_tr16_b64_v4i16((LAS s16x4*)(st + w * 8192 + swz(r0 + 4, 2 * j + (p4 >> 1)) + 8 * (p4 & 1)));
        s16x8 tt = __builtin_shufflevector(lo, hi, 0, 1, 2, 3, 4, 5, 6, 7);
        *(s16x8*)(smem + L_WO + ((w * 8 + j) * 4 + t) * 1024 + l * 16) = tt;
      }
      __syncthreads();
    }
    if (l == 0) { wait1(fl + F_TILE + h, 1u); wait1(fl + F_TILE + 32 + h, 1u); }
    asm volatile("" ::: "memory");
#pragma unroll
    for (int i = 0; i < 2; ++i) {
      int ch = tid + i * 256, r = ch >> 4, c = ch & 15;
      unsigned off = (unsigned)(r * NQKV + 4096 + h * 128 + c * 8) * 2;
      *(u32x4*)(smem + L_K + swz(8 + r, c)) = __builtin_amdgcn_raw_buffer_load_b128(wr, O_QKV + off, 0, 16);
      *(u32x4*)(smem + L_V + swz(8 + r, c)) = __builtin_amdgcn_raw_buffer_load_b128(wr, O_QKV + off + 8192, 0, 16);
    }
  }

  const float scale = 0.08838834764831845f;
  unsigned aQ[4], aK[4], aV[2], aE[2], aM, aW;
  {
    const int tid = threadIdx.x, w = tid >> 6, l = tid & 63, g = l >> 4, c16 = l & 15, q4 = c16 >> 2, p4 = c16 & 3;
#pragma unroll
    for (int t = 0; t < 4; ++t) { aQ[t] = L_Q + swz(c16 & 7, 4 * t + g); aK[t] = L_K + swz(c16, 4 * t + g); }
#pragma unroll
    for (int c = 0; c < 2; ++c) {
      aV[c] = L_V + swz(4 * g + q4, 2 * (2 * w + c) + (p4 >> 1)) + 8 * (p4 & 1);
      aE[c] = 256u * c16 + 16u * ((4 * w + 2 * c + (g >> 1)) ^ (((c16 & 3) << 2) | ((c16 >> 2) & 3))) + (g & 1) * 8;
    }
    aM = L_MEM + (c16 & 7) * 1088 + g * 16;
    aW = L_WO + w * 32768 + l * 16;
  }

  for (int s = 0; s < NSEG; ++s) {
    int tid = threadIdx.x;
    asm volatile("" : "+v"(tid));
    const int w = tid >> 6, l = tid & 63, g = l >> 4, c16 = l & 15;
    const int q4 = c16 >> 2, p4 = c16 & 3;
    unsigned spins = 0;
    STAMP(0)
    if (s == 0) {
#pragma unroll
      for (int i = 0; i < 2; ++i) {
        int ch = tid + i * 256, r = ch >> 6, c = ch & 63;
        u32x4 v = *(const u32x4*)(mem0 + r * 512 + c * 8);
        *(u32x4*)(smem + L_MEM + r * 1088 + c * 16) = v;
      }
    } else {
      u32x4 a[4];
      while (true) {
        unsigned ok = 1u;
        asm volatile("" ::: "memory");
#pragma unroll
        for (int i = 0; i < 4; ++i) a[i] = __builtin_amdgcn_raw_buffer_load_b128(wr, O_P2 + (unsigned)(s * 4096) * 4 + (i * 256 + tid) * 16, 0, 16);
#pragma unroll
        for (int i = 0; i < 4; ++i) ok &= a[i][0] & a[i][1] & a[i][2] & a[i][3];
        if (((s + 1) & 3) == 0 && s + 1 < NSEG) {
          const unsigned fo = O_FL + (unsigned)(F_TILE + ((s + 1) >> 2) * 64 + h) * 4;
          ok &= __builtin_amdgcn_raw_buffer_load_b32(wr, fo, 0, 16) & __builtin_amdgcn_raw_buffer_load_b32(wr, fo + 128, 0, 16);
        }
        if (__all(ok & 1u) || ++spins > (1u << 20)) break;
      }
#pragma unroll
      for (int i = 0; i < 4; ++i) {
        int ch = i * 256 + tid, r = ch >> 5, qy = (ch >> 2) & 7, qd = ch & 3;
        u32x2 v; v[0] = pack2(__uint_as_float(a[i][0]), __uint_as_float(a[i][1])); v[1] = pack2(__uint_as_float(a[i][2]), __uint_as_float(a[i][3]));
        *(u32x2*)(smem + L_MEM + qy * 1088 + (16 * r + 4 * qd) * 2) = v;
      }
    }
    FS(0)
    __syncthreads();
    FS(1)
    if (s > 0 && threadIdx.x == 0) __hip_atomic_fetch_add(fl + F_M + (s - 1) * 16, 1u, RLX_AGENT);
    STAMP(1)
    f32x4 acc[3][2];
#pragma unroll
    for (int m = 0; m < 3; ++m)
#pragma unroll
      for (int c = 0; c < 2; ++c) acc[m][c] = (f32x4){0, 0, 0, 0};
    {
      const char* mb = smem + aM;
      bf16x8 mf[16];
#pragma unroll
      for (int t = 0; t < 3; ++t) mf[t] = *(const bf16x8*)(mb + t * 64);
#pragma unroll
      for (int t = 0; t < 16; ++t) {
        if (t + 3 < 16) mf[t + 3] = *(const bf16x8*)(mb + (t + 3) * 64);
#pragma unroll
        for (int m = 0; m < 3; ++m)
#pragma unroll
          for (int c = 0; c < 2; ++c) acc[m][c] = __builtin_amdgcn_mfma_f32_16x16x32_bf16(wf[m][c][t], mf[t], acc[m][c], 0, 0, 0);
        __builtin_amdgcn_sched_barrier(0);
      }
    }
    FS(2)
    if (c16 < 8) {
#pragma unroll
      for (int c = 0; c < 2; ++c) {
        const unsigned off = aE[c];
        u32x2 vq, vk, vv;
        vq[0] = pack2(acc[0][c][0], acc[0][c][1]); vq[1] = pack2(acc[0][c][2], acc[0][c][3]);
        vk[0] = pack2(acc[1][c][0], acc[1][c][1]); vk[1] = pack2(acc[1][c][2], acc[1][c][3]);
        vv[0] = pack2(acc[2][c][0], acc[2][c][1]); vv[1] = pack2(acc[2][c][2], acc[2][c][3]);
        *(u32x2*)(smem + L_Q + off) = vq;
        *(u32x2*)(smem + L_K + off) = vk;
        *(u32x2*)(smem + L_V + off) = vv;
      }
    }
    FS(3)
    __syncthreads();
    FS(4)
    STAMP(2)
    f32x4 sc[3];
    s16x4 va[2][3];
    {
      bf16x8 qf[4], kf[3][4];
#pragma unroll
      for (int t = 0; t < 4; ++t) {
        qf[t] = *(const bf16x8*)(smem + aQ[t]);
        kf[0][t] = *(const bf16x8*)(smem + aK[t]);
      }
#pragma unroll
      for (int t = 0; t < 4; ++t) kf[1][t] = *(const bf16x8*)(smem + aK[t] + 4096);
      __builtin_amdgcn_sched_barrier(0);
      sc[0] = (f32x4){0, 0, 0, 0};
#pragma unroll
      for (int t = 0; t < 4; ++t) sc[0] = __builtin_amdgcn_mfma_f32_16x16x32_bf16(kf[0][t], qf[t], sc[0], 0, 0, 0);
      __builtin_amdgcn_sched_barrier(0);
#pragma unroll
      for (int t = 0; t < 4; ++t) kf[2][t] = *(const bf16x8*)(smem + aK[t] + 8192);
      sc[1] = (f32x4){0, 0, 0, 0};
#pragma unroll
      for (int t = 0; t < 4; ++t) sc[1] = __builtin_amdgcn_mfma_f32_16x16x32_bf16(kf[1][t], qf[t], sc[1], 0, 0, 0);
      __builtin_amdgcn_sched_barrier(0);
#pragma unroll
      for (int c = 0; c < 2; ++c) {
#pragma unroll
        for (int kt = 0; kt < 3; ++kt)
          va[c][kt] = __builtin_amdgcn_ds_read_tr16_b64_v4i16((LAS s16x4*)(smem + aV[c] + 4096 * kt));
      }
      sc[2] = (f32x4){0, 0, 0, 0};
#pragma unroll
      for (int t = 0; t < 4; ++t) sc[2] = __builtin_amdgcn_mfma_f32_16x16x32_bf16(kf[2][t], qf[t], sc[2], 0, 0, 0);
    }
    __builtin_amdgcn_sched_barrier(0);
    FS(5)
    bf16x8 pf0, pf1;
    {
      const bool hi = c16 >= 8;
      float xv[6];
#pragma unroll
      for (int r = 0; r < 4; ++r) xv[r] = hi ? sc[1][r] : sc[0][r];
      xv[4] = hi ? sc[2][2] : sc[2][0];
      xv[5] = hi ? sc[2][3] : sc[2][1];
      float mx = -3.0e38f;
#pragma unroll
      for (int r = 0; r < 6; ++r) {
        float v = rbf(rbf(xv[r]) * scale);
        if (r >= 4 && g >= 2) v = -3.0e38f;
        xv[r] = v;
        mx = fmaxf(mx, v);
      }
      mx = fmaxf(mx, __uint_as_float(__builtin_amdgcn_update_dpp(0, __float_as_uint(mx), 0x128, 0xf, 0xf, false)));
      {
        u32x2 r = __builtin_amdgcn_permlane16_swap(__float_as_uint(mx), __float_as_uint(mx), false, false);
        mx = fmaxf(__uint_as_float(r[0]), __uint_as_float(r[1]));
        r = __builtin_amdgcn_permlane32_swap(__float_as_uint(mx), __float_as_uint(mx), false, false);
        mx = fmaxf(__uint_as_float(r[0]), __uint_as_float(r[1]));
      }
      const float mxl = mx * 1.4426950408889634f;
      float sum = 0.f;
#pragma unroll
      for (int r = 0; r < 6; ++r) {
        float e = __builtin_amdgcn_exp2f(__builtin_fmaf(xv[r], 1.4426950408889634f, -mxl));
        if (r >= 4 && g >= 2) e = 0.f;
        xv[r] = e;
        sum += e;
      }
      sum += __uint_as_float(__builtin_amdgcn_update_dpp(0, __float_as_uint(sum), 0x128, 0xf, 0xf, false));
      {
        u32x2 r = __builtin_amdgcn_permlane16_swap(__float_as_uint(sum), __float_as_uint(sum), false, false);
        sum = __uint_as_float(r[0]) + __uint_as_float(r[1]);
        r = __builtin_amdgcn_permlane32_swap(__float_as_uint(sum), __float_as_uint(sum), false, false);
        sum = __uint_as_float(r[0]) + __uint_as_float(r[1]);
      }
      const float inv = __builtin_amdgcn_rcpf(sum);
      unsigned k0 = pack2(xv[0] * inv, xv[1] * inv), k1 = pack2(xv[2] * inv, xv[3] * inv), k2 = pack2(xv[4] * inv, xv[5] * inv);
      unsigned o0 = __builtin_amdgcn_update_dpp(0, k0, 0x128, 0xf, 0xf, false);
      unsigned o1 = __builtin_amdgcn_update_dpp(0, k1, 0x128, 0xf, 0xf, false);
      unsigned o2 = __builtin_amdgcn_update_dpp(0, k2, 0x128, 0xf, 0xf, false);
      u32x4 pb0, pb1;
      pb0[0] = k0; pb0[1] = k1; pb0[2] = o0; pb0[3] = o1;
      pb1[0] = k2; pb1[1] = o2; pb1[2] = 0; pb1[3] = 0;
      pf0 = __builtin_bit_cast(bf16x8, pb0); pf1 = __builtin_bit_cast(bf16x8, pb1);
    }
    FS(6)
    {
      f32x4 o[2];
#pragma unroll
      for (int c = 0; c < 2; ++c) {
        s16x8 v0 = __builtin_shufflevector(va[c][0], va[c][1], 0, 1, 2, 3, 4, 5, 6, 7);
        o[c] = __builtin_amdgcn_mfma_f32_16x16x32_bf16(__builtin_bit_cast(bf16x8, v0), pf0, (f32x4){0, 0, 0, 0}, 0, 0, 0);
      }
#pragma unroll
      for (int c = 0; c < 2; ++c) {
        s16x8 v1 = __builtin_shufflevector(va[c][2], va[c][2], 0, 1, 2, 3, 4, 5, 6, 7);
        o[c] = __builtin_amdgcn_mfma_f32_16x16x32_bf16(__builtin_bit_cast(bf16x8, v1), pf1, o[c], 0, 0, 0);
      }
      if (c16 < 8) {
#pragma unroll
        for (int c = 0; c < 2; ++c) {
          u32x2 v; v[0] = pack2(o[c][0], o[c][1]); v[1] = pack2(o[c][2], o[c][3]);
          *(u32x2*)(smem + L_MEM + aE[c]) = v;
        }
      }
    }
    FS(7)
    __syncthreads();
    FS(8)
    STAMP(3)
    if (s + 1 < NSEG) {
      bf16x8 of[4];
#pragma unroll
      for (int t = 0; t < 4; ++t) of[t] = *(const bf16x8*)(smem + aQ[t] + (L_MEM - L_Q));
      const char* wb = smem + aW;
      bf16x8 bw[32];
      constexpr int PD = 8;
      const unsigned base = O_P1 + (unsigned)((((s + 1) * 32 + 8 * w + (c16 >> 3)) * 32 + h) * 128 + (c16 & 7) * 16 + 4 * g) * 4;
#pragma unroll
      for (int u = 0; u < PD; ++u) bw[u] = *(const bf16x8*)(wb + u * 1024);
      f32x4 de, dod;
#pragma unroll
      for (int jp = 0; jp <= 4; ++jp) {
        f32x4 d0, d1;
        if (jp < 4) {
#pragma unroll
          for (int t = 0; t < 8; ++t) {
            const int u = jp * 8 + t;
            if (u + PD < 32) bw[u + PD] = *(const bf16x8*)(wb + (u + PD) * 1024);
            if (t == 0) d0 = __builtin_amdgcn_mfma_f32_16x16x32_bf16(bw[u], of[0], (f32x4){0, 0, 0, 0}, 0, 0, 0);
            else if (t < 4) d0 = __builtin_amdgcn_mfma_f32_16x16x32_bf16(bw[u], of[t], d0, 0, 0, 0);
            else if (t == 4) d1 = __builtin_amdgcn_mfma_f32_16x16x32_bf16(bw[u], of[0], (f32x4){0, 0, 0, 0}, 0, 0, 0);
            else d1 = __builtin_amdgcn_mfma_f32_16x16x32_bf16(bw[u], of[t - 4], d1, 0, 0, 0);
          }
        }
        if (jp > 0) {
          u32x4 v;
#pragma unroll
          for (int r = 0; r < 4; ++r)
            v[r] = 1u | (unsigned)__builtin_amdgcn_update_dpp(__float_as_uint(de[r]), __float_as_uint(dod[r]), 0x128, 0xf, 0xc, false);
          __builtin_amdgcn_raw_buffer_store_b128(v, wr, base + (jp - 1) * (2 * 32 * 128 * 4), 0, 16);
        }
        de = d0; dod = d1;
        __builtin_amdgcn_sched_barrier(0);
      }
      FS(9)
      FS(10)
      STAMP(4)
      {
        int r = tid >> 5, c = tid & 31;
        u32x4 v = *(const u32x4*)(smem + (c < 16 ? L_K : L_V) + swz(r, c & 15));
        __builtin_amdgcn_raw_buffer_store_b128(v, wr, O_KVM + (unsigned)((s * MM + r) * 8192 + (c >> 4) * 4096 + h * 128 + (c & 15) * 8) * 2, 0, 16);
      }
      f32x4 t4;
      while (true) {
        u32x4 a[4]; unsigned ok = 1u;
        asm volatile("" ::: "memory");
#pragma unroll
        for (int i = 0; i < 4; ++i)
          a[i] = __builtin_amdgcn_raw_buffer_load_b128(wr, O_P1 + (unsigned)((((s + 1) * 32 + h) * 32 + i * 8 + (l >> 3)) * 128 + 32 * w + (l & 7) * 4) * 4, 0, 16);
        t4 = (f32x4){0, 0, 0, 0};
#pragma unroll
        for (int i = 0; i < 4; ++i) {
          ok &= a[i][0] & a[i][1] & a[i][2] & a[i][3];
#pragma unroll
          for (int r = 0; r < 4; ++r) t4[r] += __uint_as_float(a[i][r]);
        }
        if (__all(ok & 1u) || ++spins > (1u << 20)) break;
      }
      FS(11)
      STAMP(5)
      u32x4 v;
#pragma unroll
      for (int r = 0; r < 4; ++r) {
        float x = t4[r];
        x += __uint_as_float(__builtin_amdgcn_update_dpp(0, __float_as_uint(x), 0x128  , 0xf, 0xf, false));
        u32x2 q = __builtin_amdgcn_permlane16_swap(__float_as_uint(x), __float_as_uint(x), false, false);
        x = __uint_as_float(q[0]) + __uint_as_float(q[1]);
        q = __builtin_amdgcn_permlane32_swap(__float_as_uint(x), __float_as_uint(x), false, false);
        x = __uint_as_float(q[0]) + __uint_as_float(q[1]);
        v[r] = __float_as_uint(x) | 1u;
      }
      if (l < 8) __builtin_amdgcn_raw_buffer_store_b128(v, wr, O_P2 + (unsigned)((s + 1) * 4096 + h * 128 + 32 * w + l * 4) * 4, 0, 16);
      {
        const int wu = __builtin_amdgcn_readfirstlane(w);
#pragma unroll
        for (int i = 0; i < 2; ++i) {
          const int r4 = wu * 8 + i * 4;
          const int rr = r4 + (l >> 4), row = 8 + rr;
          const int cc = (l & 15) ^ (((row & 3) << 2) | ((row >> 2) & 3));
          const char* src = (const char*)p.qkv + ((size_t)((s + 1) * SEG + rr) * NQKV + 4096 + h * 128 + cc * 8) * 2;
          __builtin_amdgcn_global_load_lds((const void*)src, (LAS void*)(smem + L_K + (8 + r4) * 256), 16, 0, 16);
          __builtin_amdgcn_global_load_lds((const void*)(src + 8192), (LAS void*)(smem + L_V + (8 + r4) * 256), 16, 0, 16);
        }
      }
      FS(12)
      STAMP(6)
    } else {
      int r = tid >> 5, c = tid & 31;
      u32x4 v = *(const u32x4*)(smem + (c < 16 ? L_K : L_V) + swz(r, c & 15));
      __builtin_amdgcn_raw_buffer_store_b128(v, wr, O_KVM + (unsigned)((s * MM + r) * 8192 + (c >> 4) * 4096 + h * 128 + (c & 15) * 8) * 2, 0, 16);
      asm volatile("s_waitcnt vmcnt(0)" ::: "memory");
      __syncthreads();
      if (threadIdx.x == 0) __hip_atomic_fetch_add(fl + F_M + s * 16, 1u, RLX_AGENT);
    }
  }
}

__device__ __forceinline__ void task_attn(const Params& p, char* smem, int s, int hg) {
  const int tid = threadIdx.x, w = tid >> 6, l = tid & 63, g = l >> 4, c16 = l & 15;
  const int q4 = c16 >> 2, p4 = c16 & 3;
  typedef __attribute__((ext_vector_type(8))) short s16x8;
  const u16* __restrict__ qkv = p.qkv;
  const u16* __restrict__ kvm = p.kvm;
  const float scale = 0.08838834764831845f;
  char* sv = smem + w * 48 * 256;
  const int h = hg * 4 + w;
  u32x4 vst[12];
#pragma unroll
  for (int i = 0; i < 12; ++i) {
    int ch = l + i * 64, r = ch >> 4, c = ch & 15;
    u32x4 v = (u32x4){0, 0, 0, 0};
    if (r < 8) v = *(const u32x4*)(kvm + (size_t)(s * MM + r) * 8192 + 4096 + h * 128 + c * 8);
    else if (r < 40) v = *(const u32x4*)(qkv + (size_t)(s * SEG + r - 8) * NQKV + 8192 + h * 128 + c * 8);
    vst[i] = v;
  }
  bf16x8 qf[4][2], kf[4][3];
#pragma unroll
  for (int t = 0; t < 4; ++t) {
#pragma unroll
    for (int qt = 0; qt < 2; ++qt)
      qf[t][qt] = *(const bf16x8*)(qkv + (size_t)(s * SEG + 16 * qt + c16) * NQKV + h * 128 + 32 * t + 8 * g);
#pragma unroll
    for (int kt = 0; kt < 3; ++kt) {
      int key = 16 * kt + c16;
      const u16* src = key < 8 ? kvm + (size_t)(s * MM + key) * 8192 + h * 128
                               : qkv + (size_t)(s * SEG + (key < 40 ? key - 8 : 0)) * NQKV + 4096 + h * 128;
      kf[t][kt] = *(const bf16x8*)(src + 32 * t + 8 * g);
    }
  }
#pragma unroll
  for (int i = 0; i < 12; ++i) {
    int ch = l + i * 64, r = ch >> 4, c = ch & 15;
    *(u32x4*)(sv + swz(r, c)) = vst[i];
  }
  f32x4 sc[3][2];
#pragma unroll
  for (int kt = 0; kt < 3; ++kt)
#pragma unroll
    for (int qt = 0; qt < 2; ++qt) sc[kt][qt] = (f32x4){0, 0, 0, 0};
#pragma unroll
  for (int t = 0; t < 4; ++t)
#pragma unroll
    for (int kt = 0; kt < 3; ++kt)
#pragma unroll
      for (int qt = 0; qt < 2; ++qt) sc[kt][qt] = __builtin_amdgcn_mfma_f32_16x16x32_bf16(kf[t][kt], qf[t][qt], sc[kt][qt], 0, 0, 0);
  bf16x8 pf0[2], pf1[2];
#pragma unroll
  for (int qt = 0; qt < 2; ++qt) {
    int qi = 8 + 16 * qt + c16;
    float mx = -3.0e38f;
#pragma unroll
    for (int kt = 0; kt < 3; ++kt)
#pragma unroll
      for (int r = 0; r < 4; ++r) {
        int key = 16 * kt + 4 * g + r;
        float v = rbf(rbf(sc[kt][qt][r]) * scale);
        if (key > qi) v = -1e9f;
        if (key >= 40) v = -3.0e38f;
        sc[kt][qt][r] = v;
        mx = fmaxf(mx, v);
      }
    mx = fmaxf(mx, __shfl_xor(mx, 16));
    mx = fmaxf(mx, __shfl_xor(mx, 32));
    float sum = 0.f;
#pragma unroll
    for (int kt = 0; kt < 3; ++kt)
#pragma unroll
      for (int r = 0; r < 4; ++r) {
        int key = 16 * kt + 4 * g + r;
        float e = (key >= 40) ? 0.f : __expf(sc[kt][qt][r] - mx);
        sc[kt][qt][r] = e;
        sum += e;
      }
    sum += __shfl_xor(sum, 16);
    sum += __shfl_xor(sum, 32);
    float inv = 1.f / sum;
    u32x4 b0, b1;
    b0[0] = pack2(sc[0][qt][0] * inv, sc[0][qt][1] * inv); b0[1] = pack2(sc[0][qt][2] * inv, sc[0][qt][3] * inv);
    b0[2] = pack2(sc[1][qt][0] * inv, sc[1][qt][1] * inv); b0[3] = pack2(sc[1][qt][2] * inv, sc[1][qt][3] * inv);
    b1[0] = pack2(sc[2][qt][0] * inv, sc[2][qt][1] * inv); b1[1] = pack2(sc[2][qt][2] * inv, sc[2][qt][3] * inv);
    b1[2] = 0; b1[3] = 0;
    pf0[qt] = __builtin_bit_cast(bf16x8, b0);
    pf1[qt] = __builtin_bit_cast(bf16x8, b1);
  }
  __builtin_amdgcn_s_waitcnt(0xc07f);
  __builtin_amdgcn_wave_barrier();
#pragma unroll
  for (int c = 0; c < 8; ++c) {
    int ch = 2 * c + (p4 >> 1);
    s16x4 a0 = __builtin_amdgcn_ds_read_tr16_b64_v4i16((LAS s16x4*)(sv + swz(4 * g + q4, ch) + 8 * (p4 & 1)));
    s16x4 a1 = __builtin_amdgcn_ds_read_tr16_b64_v4i16((LAS s16x4*)(sv + swz(16 + 4 * g + q4, ch) + 8 * (p4 & 1)));
    s16x4 a2 = __builtin_amdgcn_ds_read_tr16_b64_v4i16((LAS s16x4*)(sv + swz(32 + 4 * g + q4, ch) + 8 * (p4 & 1)));
    s16x8 v0 = __builtin_shufflevector(a0, a1, 0, 1, 2, 3, 4, 5, 6, 7);
    s16x8 v1 = __builtin_shufflevector(a2, a2, 0, 1, 2, 3, 4, 5, 6, 7);
#pragma unroll
    for (int qt = 0; qt < 2; ++qt) {
      f32x4 o = (f32x4){0, 0, 0, 0};
      o = __builtin_amdgcn_mfma_f32_16x16x32_bf16(__builtin_bit_cast(bf16x8, v0), pf0[qt], o, 0, 0, 0);
      o = __builtin_amdgcn_mfma_f32_16x16x32_bf16(__builtin_bit_cast(bf16x8, v1), pf1[qt], o, 0, 0, 0);
      u32x2 v; v[0] = pack2(o[0], o[1]); v[1] = pack2(o[2], o[3]);
      st8_sc1(p.ox + (size_t)(s * SEG + 16 * qt + c16) * 4096 + h * 128 + 16 * c + 4 * g, v);
    }
  }
}

static constexpr int E_WO = 0;
static constexpr int E_RED = 131072;
__device__ __forceinline__ void load_wo_slice(const Params& p, char* smem, int cb) {
  const int tid = threadIdx.x, w = tid >> 6, l = tid & 63, g = l >> 4, c16 = l & 15;
  const int q4 = c16 >> 2, p4 = c16 & 3;
  typedef __attribute__((ext_vector_type(8))) short s16x8;
  char* st = smem + E_RED;
  for (int r8 = 0; r8 < 8; ++r8) {
    u32x4 v[4];
#pragma unroll
    for (int i = 0; i < 4; ++i) {
      int ch = tid + i * 256, r = ch >> 1, c = ch & 1;
      v[i] = *(const u32x4*)(p.Wo + (size_t)(r8 * 512 + r) * DM + cb * 16 + c * 8);
    }
#pragma unroll
    for (int i = 0; i < 4; ++i) {
      int ch = tid + i * 256, r = ch >> 1, c = ch & 1;
      *(u32x4*)(st + r * 32 + c * 16) = v[i];
    }
    __syncthreads();
#pragma unroll
    for (int t = 0; t < 4; ++t) {
      int ks = 4 * w + t;
      int r0 = 32 * ks + 8 * g + q4;
      s16x4 lo = __builtin_amdgcn_ds_read_tr16_b64_v4i16((LAS s16x4*)(st + r0 * 32 + p4 * 8));
      s16x4 hi = __builtin_amdgcn_ds_read_tr16_b64_v4i16((LAS s16x4*)(st + (r0 + 4) * 32 + p4 * 8));
      s16x8 tt = __builtin_shufflevector(lo, hi, 0, 1, 2, 3, 4, 5, 6, 7);
      *(s16x8*)(smem + E_WO + (r8 * 16 + ks) * 1024 + l * 16) = tt;
    }
    __syncthreads();
  }
}
__device__ __forceinline__ void task_out(const Params& p, char* smem, int s, int cb) {
  const int tid = threadIdx.x, w = tid >> 6, l = tid & 63, g = l >> 4, c16 = l & 15;
  f32x4 acc[2];
  acc[0] = (f32x4){0, 0, 0, 0}; acc[1] = (f32x4){0, 0, 0, 0};
  const u16* a0 = p.ox + (size_t)(s * SEG + c16) * 4096 + w * 1024 + 8 * g;
  const u16* a1 = a0 + 16 * 4096;
  const char* wb = smem + E_WO + w * 32768 + l * 16;
#pragma unroll
  for (int hb = 0; hb < 2; ++hb) {
    bf16x8 x0[16], x1[16];
#pragma unroll
    for (int t = 0; t < 16; ++t) { x0[t] = *(const bf16x8*)(a0 + 32 * (hb * 16 + t)); x1[t] = *(const bf16x8*)(a1 + 32 * (hb * 16 + t)); }
    __builtin_amdgcn_sched_barrier(0);
#pragma unroll
    for (int t = 0; t < 16; ++t) {
      bf16x8 wf = *(const bf16x8*)(wb + (hb * 16 + t) * 1024);
      acc[0] = __builtin_amdgcn_mfma_f32_16x16x32_bf16(wf, x0[t], acc[0], 0, 0, 0);
      acc[1] = __builtin_amdgcn_mfma_f32_16x16x32_bf16(wf, x1[t], acc[1], 0, 0, 0);
    }
    __builtin_amdgcn_sched_barrier(0);
  }
  *(f32x4*)(smem + E_RED + (w * 2 + 0) * 1024 + l * 16) = acc[0];
  *(f32x4*)(smem + E_RED + (w * 2 + 1) * 1024 + l * 16) = acc[1];
  __syncthreads();
  if (w < 2) {
    f32x4 a = *(f32x4*)(smem + E_RED + w * 1024 + l * 16);
#pragma unroll
    for (int i = 1; i < 4; ++i) a += *(f32x4*)(smem + E_RED + (i * 2 + w) * 1024 + l * 16);
    int m = s * SEG + 16 * w + c16, n = cb * 16 + 4 * g;
    u32x2 gg = *(const u32x2*)(p.g + (size_t)m * DM + n), xx = *(const u32x2*)(p.x + (size_t)m * DM + n);
    float gf[4] = {bf2f(gg[0] & 0xffff), bf2f(gg[0] >> 16), bf2f(gg[1] & 0xffff), bf2f(gg[1] >> 16)};
    float xf[4] = {bf2f(xx[0] & 0xffff), bf2f(xx[0] >> 16), bf2f(xx[1] & 0xffff), bf2f(xx[1] >> 16)};
    float r[4];
#pragma unroll
    for (int i = 0; i < 4; ++i) r[i] = xf[i] + rbf(rbf(a[i]) * gf[i]);
    u32x2 o; o[0] = pack2(r[0], r[1]); o[1] = pack2(r[2], r[3]);
    *(u32x2*)(p.out + (size_t)m * DM + n) = o;
  }
  __syncthreads();
}

__device__ __forceinline__ void worker(const Params& p, char* smem) {
  constexpr int NWK = 224;
  const int wid = blockIdx.x - NH;
  for (int t = wid; t < 1024; t += NWK) {
    int rb = t >> 6, ct = t & 63;
    const u16* B = ct < 32 ? p.Wk : p.Wv;
    EpiStore epi{p.qkv + 4096 + (ct >> 5) * 4096, NQKV, 0};
    gemm_tile<128, 128, 64, EpiStore>(smem, p.xn, DM, B, 4096, S_, 4096, DM, rb * 128, (ct & 31) * 128, epi);
    publish(p.fl + F_TILE + t, p.fl + F_QKV + rb * 16);
  }
  for (int t = (wid + 224 - (1024 % NWK)) % NWK; t < 176; t += NWK) {
    int rb = t / 11, ct = t % 11;
    EpiGelu epi{p.h1, FF, 0, p.b1};
    gemm_tile<128, 128, 64, EpiGelu>(smem, p.y, DM, p.w1, FF, S_, FF, DM, rb * 128, ct * 128, epi);
    publish(p.fl + F_H + rb * 16, nullptr);
  }
  for (int t = (wid + 48) % NWK; t < 512; t += NWK) {
    int rb = t >> 5, ct = t & 31;
    EpiStore epi{p.qkv, NQKV, 0};
    gemm_tile<128, 128, 64, EpiStore>(smem, p.xn, DM, p.Wq, 4096, S_, 4096, DM, rb * 128, ct * 128, epi);
    publish(p.fl + F_QKV + rb * 16, nullptr);
  }
  for (int t = (wid + 120) % NWK; t < 64; t += NWK) {
    int rb = t >> 2, ct = t & 3;
    if (threadIdx.x == 0) wait1<64>(p.fl + F_H + rb * 16, 11u);
    acquire_all();
    EpiSilu epi{p.g, DM, 0, p.b2};
    gemm_tile<128, 128, 64, EpiSilu>(smem, p.h1, FF, p.w2, DM, S_, DM, FF, rb * 128, ct * 128, epi);
    publish(p.fl + F_G + rb * 16, nullptr);
  }
  if (wid < 32) {
    for (int s = wid >> 3; s < NSEG; s += 4) {
      if (threadIdx.x == 0) {
        wait1<64>(p.fl + F_QKV + (s >> 2) * 16, 96u);
        if (s >= 60) wait1<8>(p.fl + F_M + s * 16, 32u); else wait1<64>(p.fl + F_M + s * 16, 32u);
      }
      acquire_all();
      task_attn(p, smem, s, wid & 7);
      asm volatile("s_waitcnt vmcnt(0)" ::: "memory");
      __syncthreads();
      if (threadIdx.x == 0) {
        unsigned old = __hip_atomic_fetch_add(p.fl + F_OX + s * 16, 1u, RLX_AGENT);
        if (old == 7u)
          for (int c = 0; c < 32; ++c) __hip_atomic_store(p.fl + F_OXB + (s * 32 + c) * 16, 1u, RLX_AGENT);
      }
    }
  } else {
    const int j = wid - 32, cb = j & 31, grp = j >> 5;
    load_wo_slice(p, smem, cb);
    for (int s = grp; s < NSEG; s += 6) {
      if (threadIdx.x == 0) {
        wait1<64>(p.fl + F_G + (s >> 2) * 16, 4u);
        if (s >= 60) wait1<8>(p.fl + F_OXB + (s * 32 + cb) * 16, 1u); else wait1<64>(p.fl + F_OXB + (s * 32 + cb) * 16, 1u);
      }
      acquire_all();
      task_out(p, smem, s, cb);
    }
  }
}

__global__ __launch_bounds__(256, 1) void k_main(Params p) {
  extern __shared__ __attribute__((aligned(16))) char smem[];
  if (blockIdx.x < NH) recurrence(p, smem);
  else worker(p, smem);
}

extern "C" void kernel_launch(void* const* d_in, const int* in_sizes, int n_in, void* d_out, int out_size, void* d_ws,
                              size_t ws_size, hipStream_t stream) {
  Params p;
  p.x = (const u16*)d_in[0];
  p.mem0 = (const u16*)d_in[1];
  p.Wq = (const u16*)d_in[2];
  p.Wk = (const u16*)d_in[3];
  p.Wv = (const u16*)d_in[4];
  p.Wo = (const u16*)d_in[5];
  p.w1 = (const u16*)d_in[8];
  p.b1 = (const u16*)d_in[9];
  p.w2 = (const u16*)d_in[10];
  p.b2 = (const u16*)d_in[11];
  p.out = (u16*)d_out;
  char* ws = (char*)d_ws;
  size_t o = 0;
  auto take = [&](size_t bytes) { char* q = ws + o; o += (bytes + 255) & ~(size_t)255; return q; };
  p.fl = (unsigned*)take(F_WORDS * 4);
  p.P2 = (float*)take((size_t)NSEG * 4096 * 4);
  p.P1 = (float*)take((size_t)NSEG * 32 * 32 * 128 * 4);
  const size_t zbytes = o;
  p.xn = (u16*)take((size_t)S_ * DM * 2);
  p.y = (u16*)take((size_t)S_ * DM * 2);
  p.qkv = (u16*)take((size_t)S_ * NQKV * 2);
  p.h1 = (u16*)take((size_t)S_ * FF * 2);
  p.g = (u16*)take((size_t)S_ * DM * 2);
  p.kvm = (u16*)take((size_t)NSEG * MM * 8192 * 2);
  p.ox = (u16*)take((size_t)S_ * 4096 * 2);
  p.stamps = (unsigned long long*)(ws + 200 * 1024 * 1024);

  k_prep<<<512, 256, 0, stream>>>(p.x, p.xn, p.y, (float*)ws, (int)(zbytes / 16));
  hipFuncSetAttribute((const void*)k_main, hipFuncAttributeMaxDynamicSharedMemorySize, 163840);
  k_main<<<256, 256, 163840, stream>>>(p);
}
```

```cpp
#include <hip/hip_runtime.h>
#include <hip/hip_bf16.h>
#include <stdint.h>

typedef __attribute__((ext_vector_type(8))) __bf16 bf16x8;
typedef __attribute__((ext_vector_type(4))) __bf16 bf16x4;
typedef __attribute__((ext_vector_type(4))) float f32x4;
typedef __attribute__((ext_vector_type(4))) short s16x4;
typedef __attribute__((ext_vector_type(4))) unsigned u32x4;
typedef __attribute__((ext_vector_type(2))) unsigned u32x2;
typedef unsigned short u16;
#define LAS __attribute__((address_space(3)))

static constexpr int S_ = 2048, DM = 512, NH = 32, DH = 128, MM = 8, SEG = 32, NSEG = 64, FF = 1376;
static constexpr int NQKV = 3 * NH * DH;

__device__ __forceinline__ float bf2f(u16 v) { return __uint_as_float(((unsigned)v) << 16); }
__device__ __forceinline__ u16 f2bf(float f) {
  __bf16 b = (__bf16)f;
  return __builtin_bit_cast(u16, b);
}
__device__ __forceinline__ float rbf(float f) { return bf2f(f2bf(f)); }
typedef __attribute__((ext_vector_type(2))) float f32x2;
typedef __attribute__((ext_vector_type(2))) __bf16 bf16x2;
__device__ __forceinline__ unsigned pack2(float a, float b) {
  bf16x2 r = __builtin_convertvector((f32x2){a, b}, bf16x2);
  return __builtin_bit_cast(unsigned, r);
}

__global__ __launch_bounds__(256) void k_prep(const u16* __restrict__ x, u16* __restrict__ xn, u16* __restrict__ y,
                                              float* __restrict__ zb, int zn4) {
  int tid = threadIdx.x, w = tid >> 6, l = tid & 63;
  int row = blockIdx.x * 4 + w;
  {
    int gt = blockIdx.x * 256 + tid;
    for (int i = gt; i < zn4; i += 512 * 256) ((f32x4*)zb)[i] = (f32x4){0, 0, 0, 0};
  }
  u32x4 raw = *(const u32x4*)(x + (size_t)row * DM + l * 8);
  float v[8];
#pragma unroll
  for (int i = 0; i < 4; ++i) { v[2 * i] = bf2f(raw[i] & 0xffff); v[2 * i + 1] = bf2f(raw[i] >> 16); }
#pragma unroll
  for (int pass = 0; pass < 2; ++pass) {
    float s = 0.f;
#pragma unroll
    for (int i = 0; i < 8; ++i) s += v[i];
#pragma unroll
    for (int m = 32; m >= 1; m >>= 1) s += __shfl_xor(s, m);
    float mean = s * (1.f / DM);
    float q = 0.f;
#pragma unroll
    for (int i = 0; i < 8; ++i) { float d = v[i] - mean; q += d * d; }
#pragma unroll
    for (int m = 32; m >= 1; m >>= 1) q += __shfl_xor(q, m);
    float var = rbf(q * (1.f / DM));
    float mb = rbf(mean);
    float den = rbf(sqrtf(rbf(var + 1e-5f)));
#pragma unroll
    for (int i = 0; i < 8; ++i) v[i] = rbf(rbf(v[i] - mb) / den);
    u32x4 o;
#pragma unroll
    for (int i = 0; i < 4; ++i) o[i] = pack2(v[2 * i], v[2 * i + 1]);
    *(u32x4*)((pass == 0 ? xn : y) + (size_t)row * DM + l * 8) = o;
  }
}

struct Params {
  const u16 *x, *mem0, *Wq, *Wk, *Wv, *Wo, *w1, *b1, *w2, *b2;
  u16 *out, *xn, *y, *qkv, *h1, *g, *kvm, *ox;
  float *P1, *P2;
  unsigned* fl;
  unsigned long long* stamps;
};
#define RLX_AGENT __ATOMIC_RELAXED, __HIP_MEMORY_SCOPE_AGENT
static constexpr int F_TILE = 0;
static constexpr int F_QKV = 1024;
static constexpr int F_H = 1280;
static constexpr int F_G = 1536;
static constexpr int F_M = 2048;
static constexpr int F_OX = 3072;
static constexpr int F_OXB = 4096;
static constexpr int F_WORDS = 4096 + 64 * 32 * 16;
static constexpr unsigned O_FL = 0;
static constexpr unsigned O_P2 = F_WORDS * 4;
static constexpr unsigned O_P1 = O_P2 + NSEG * 4096 * 4;
static constexpr unsigned O_ZEND = O_P1 + NSEG * 32 * 32 * 128 * 4;
static constexpr unsigned O_XN = O_ZEND;
static constexpr unsigned O_Y = O_XN + S_ * DM * 2;
static constexpr unsigned O_QKV = O_Y + S_ * DM * 2;
static constexpr unsigned O_H1 = O_QKV + S_ * NQKV * 2;
static constexpr unsigned O_G = O_H1 + S_ * FF * 2;
static constexpr unsigned O_KVM = O_G + S_ * DM * 2;
static constexpr unsigned O_END = O_KVM + NSEG * MM * 8192 * 2;

__device__ __forceinline__ void st8_sc1(u16* p, u32x2 v) {
  __hip_atomic_store((unsigned long long*)p, (unsigned long long)v[0] | ((unsigned long long)v[1] << 32), RLX_AGENT);
}
__device__ __forceinline__ void publish(unsigned* c1, unsigned* c2) {
  asm volatile("s_waitcnt vmcnt(0)" ::: "memory");
  __syncthreads();
  if (threadIdx.x == 0) {
    __hip_atomic_fetch_add(c1, 1u, RLX_AGENT);
    if (c2) __hip_atomic_fetch_add(c2, 1u, RLX_AGENT);
  }
}
template <int SLP = 4>
__device__ __forceinline__ void wait1(unsigned* c, unsigned target) {
  unsigned spins = 0;
  while (__hip_atomic_load(c, RLX_AGENT) < target) {
    __builtin_amdgcn_s_sleep(SLP);
    if (++spins > (1u << 22)) break;
  }
}
__device__ __forceinline__ void acquire_all() {
  if (threadIdx.x == 0) {
    __builtin_amdgcn_fence(__ATOMIC_ACQUIRE, "agent");
    asm volatile("s_waitcnt vmcnt(0)" ::: "memory");
  }
  __syncthreads();
}

struct EpiStore {
  u16* C; int ldc; int pad_;
  __device__ __forceinline__ void operator()(int m, int n, f32x4 a) const {
    u32x2 o; o[0] = pack2(a[0], a[1]); o[1] = pack2(a[2], a[3]);
    st8_sc1(C + (size_t)m * ldc + n, o);
  }
};
struct EpiGelu {
  u16* C; int ldc; int pad_; const u16* b;
  __device__ __forceinline__ void operator()(int m, int n, f32x4 a) const {
    float r[4];
#pragma unroll
    for (int i = 0; i < 4; ++i) {
      float v = rbf(rbf(a[i]) + bf2f(b[n + i]));
      float u = 0.7978845608028654f * (v + 0.044715f * v * v * v);
      r[i] = 0.5f * v * (1.f + tanhf(u));
    }
    u32x2 o; o[0] = pack2(r[0], r[1]); o[1] = pack2(r[2], r[3]);
    st8_sc1(C + (size_t)m * ldc + n, o);
  }
};
struct EpiSilu {
  u16* C; int ldc; int pad_; const u16* b;
  __device__ __forceinline__ void operator()(int m, int n, f32x4 a) const {
    float r[4];
#pragma unroll
    for (int i = 0; i < 4; ++i) {
      float v = rbf(rbf(a[i]) + bf2f(b[n + i]));
      r[i] = v / (1.f + __expf(-v));
    }
    u32x2 o; o[0] = pack2(r[0], r[1]); o[1] = pack2(r[2], r[3]);
    st8_sc1(C + (size_t)m * ldc + n, o);
  }
};

template <int BM, int BN, int BK>
struct GemmCfg {
  static constexpr int ASTR = BK * 2 + 16;
  static constexpr int BSTR = BN * 2 + 32;
  static constexpr int ABYTES = BM * ASTR, BBYTES = BK * BSTR;
  static constexpr int LDS = 2 * (ABYTES + BBYTES);
};

template <int BM, int BN, int BK, class Epi>
__device__ __forceinline__ void gemm_tile(char* smem, const u16* __restrict__ A, int lda, const u16* __restrict__ B, int ldb,
                                          int M, int N, int K, int bm, int bn, const Epi& epi) {
  using C = GemmCfg<BM, BN, BK>;
  constexpr int WM = BM / 2, WN = BN / 2, TM = WM / 16, TN = WN / 16;
  constexpr int ACH = BM * BK / 8 / 256;
  constexpr int BCH = BK * BN / 8 / 256;
  static_assert(ACH >= 1 && BCH >= 1, "tile too small");
  const int tid = threadIdx.x, w = tid >> 6, l = tid & 63, g = l >> 4, c16 = l & 15;
  const int wm = w >> 1, wn = w & 1;
  f32x4 acc[TM][TN];
#pragma unroll
  for (int i = 0; i < TM; ++i)
#pragma unroll
    for (int j = 0; j < TN; ++j) acc[i][j] = (f32x4){0, 0, 0, 0};
  u32x4 ra[ACH], rb[BCH];
  auto gload = [&](int k0) {
#pragma unroll
    for (int i = 0; i < ACH; ++i) {
      int ch = tid + i * 256, r = ch / (BK / 8), c = ch % (BK / 8);
      int k = k0 + c * 8;
      ra[i] = (k < K) ? *(const u32x4*)(A + (size_t)(bm + r) * lda + k) : (u32x4){0, 0, 0, 0};
    }
#pragma unroll
    for (int i = 0; i < BCH; ++i) {
      int ch = tid + i * 256, r = ch / (BN / 8), c = ch % (BN / 8);
      int k = k0 + r, n = bn + c * 8;
      rb[i] = (k < K && n < N) ? *(const u32x4*)(B + (size_t)k * ldb + n) : (u32x4){0, 0, 0, 0};
    }
  };
  auto lstore = [&](int buf) {
    char* sa = smem + buf * (C::ABYTES + C::BBYTES);
    char* sb = sa + C::ABYTES;
#pragma unroll
    for (int i = 0; i < ACH; ++i) {
      int ch = tid + i * 256, r = ch / (BK / 8), c = ch % (BK / 8);
      *(u32x4*)(sa + r * C::ASTR + c * 16) = ra[i];
    }
#pragma unroll
    for (int i = 0; i < BCH; ++i) {
      int ch = tid + i * 256, r = ch / (BN / 8), c = ch % (BN / 8);
      *(u32x4*)(sb + r * C::BSTR + ((c ^ (((r >> 3) & 1) * (BN / 16))) * 16)) = rb[i];
    }
  };
  const int nk = (K + BK - 1) / BK;
  gload(0);
  lstore(0);
  __syncthreads();
  for (int kt = 0; kt < nk; ++kt) {
    int buf = kt & 1;
    if (kt + 1 < nk) gload((kt + 1) * BK);
    const char* sa = smem + buf * (C::ABYTES + C::BBYTES);
    const char* sb = sa + C::ABYTES;
#pragma unroll
    for (int ks = 0; ks < BK / 32; ++ks) {
      bf16x8 af[TM], bfr[TN];
#pragma unroll
      for (int i = 0; i < TM; ++i)
        af[i] = *(const bf16x8*)(sa + (wm * WM + i * 16 + c16) * C::ASTR + (ks * 32 + g * 8) * 2);
#pragma unroll
      for (int j = 0; j < TN; ++j) {
        int q = c16 >> 2, p = c16 & 3;
        int col = wn * WN + j * 16 + p * 4;
        s16x4 lo, hi;
        {
          int r = ks * 32 + g * 8 + q;
          lo = __builtin_amdgcn_ds_read_tr16_b64_v4i16((LAS s16x4*)(sb + r * C::BSTR + ((col * 2) ^ (((r >> 3) & 1) * BN))));
          r += 4;
          hi = __builtin_amdgcn_ds_read_tr16_b64_v4i16((LAS s16x4*)(sb + r * C::BSTR + ((col * 2) ^ (((r >> 3) & 1) * BN))));
        }
        typedef __attribute__((ext_vector_type(8))) short s16x8;
        s16x8 t = __builtin_shufflevector(lo, hi, 0, 1, 2, 3, 4, 5, 6, 7);
        bfr[j] = __builtin_bit_cast(bf16x8, t);
      }
#pragma unroll
      for (int i = 0; i < TM; ++i)
#pragma unroll
        for (int j = 0; j < TN; ++j) acc[i][j] = __builtin_amdgcn_mfma_f32_16x16x32_bf16(bfr[j], af[i], acc[i][j], 0, 0, 0);
    }
    if (kt + 1 < nk) lstore(buf ^ 1);
    __syncthreads();
  }
#pragma unroll
  for (int i = 0; i < TM; ++i)
#pragma unroll
    for (int j = 0; j < TN; ++j) {
      int m = bm + wm * WM + i * 16 + c16, n = bn + wn * WN + j * 16 + g * 4;
      if (n < N) epi(m, n, acc[i][j]);
    }
}

__device__ __forceinline__ unsigned swz(unsigned row, unsigned ch) {
  return 256u * row + 16u * (ch ^ (((row & 3) << 2) | ((row >> 2) & 3)));
}

static constexpr int L_WO = 0;
static constexpr int L_V = 131072;
static constexpr int L_K = L_V + 40 * 256;
static constexpr int L_Q = L_K + 40 * 256;
static constexpr int L_MEM = L_Q + 8 * 256;
static constexpr int L_END = L_MEM + 8704;
static_assert(L_END <= 163840, "lds");

#define FS(i)
#define STAMP(i)

__device__ __forceinline__ void recurrence(const Params& p, char* smem) {
  const u16* __restrict__ Wq = p.Wq; const u16* __restrict__ Wk = p.Wk; const u16* __restrict__ Wv = p.Wv;
  const u16* __restrict__ Wo = p.Wo; const u16* __restrict__ mem0 = p.mem0;
  unsigned long long* stamps = p.stamps;
  const int h = blockIdx.x;
  const __amdgpu_buffer_rsrc_t wr = __builtin_amdgcn_make_buffer_rsrc(p.fl, 0, O_END, 0x00020000);
  unsigned* const fl = p.fl;
  typedef __attribute__((ext_vector_type(8))) short s16x8;

  bf16x8 wf[3][2][16];
  {
    const int tid = threadIdx.x, w = tid >> 6, l = tid & 63, g = l >> 4, c16 = l & 15;
    const int q4 = c16 >> 2, p4 = c16 & 3;
#pragma unroll
    for (int m = 0; m < 3; ++m) {
      const u16* W = m == 0 ? Wq : (m == 1 ? Wk : Wv);
      {
        const int wu = __builtin_amdgcn_readfirstlane(w);
#pragma unroll
        for (int i = 0; i < 32; ++i) {
          const int r4 = (i * 4 + wu) * 4;
          const int row = r4 + (l >> 4);
          const int cc = (l & 15) ^ (((row & 3) << 2) | ((row >> 2) & 3));
          __builtin_amdgcn_global_load_lds((const void*)(W + (size_t)row * 4096 + h * 128 + cc * 8), (LAS void*)(smem + r4 * 256), 16, 0, 0);
        }
      }
      __syncthreads();
#pragma unroll
      for (int c = 0; c < 2; ++c)
#pragma unroll
        for (int t = 0; t < 16; ++t) {
          int ct = 2 * w + c;
          int r0 = 32 * t + 8 * g + q4;
          s16x4 lo = __builtin_amdgcn_ds_read_tr16_b64_v4i16((LAS s16x4*)(smem + swz(r0, 2 * ct + (p4 >> 1)) + 8 * (p4 & 1)));
          s16x4 hi = __builtin_amdgcn_ds_read_tr16_b64_v4i16((LAS s16x4*)(smem + swz(r0 + 4, 2 * ct + (p4 >> 1)) + 8 * (p4 & 1)));
          s16x8 tt = __builtin_shufflevector(lo, hi, 0, 1, 2, 3, 4, 5, 6, 7);
          wf[m][c][t] = __builtin_bit_cast(bf16x8, tt);
        }
      __syncthreads();
    }
    u32x4 v[8];
#pragma unroll
    for (int i = 0; i < 8; ++i) {
      int ch = tid + i * 256, r = ch >> 6, c = ch & 63;
      v[i] = *(const u32x4*)(Wo + (size_t)(h * 128 + r) * 512 + c * 8);
    }
    for (int t = 0; t < 4; ++t) {
      char* st = smem + 131072;
#pragma unroll
      for (int i = 0; i < 8; ++i) {
        int ch = tid + i * 256, r = ch >> 6, c = ch & 63;
        *(u32x4*)(st + (c >> 4) * 8192 + swz(r, c & 15)) = v[i];
      }
      if (t + 1 < 4) {
#pragma unroll
        for (int i = 0; i < 8; ++i) {
          int ch = tid + i * 256, r = ch >> 6, c = ch & 63;
          v[i] = *(const u32x4*)(Wo + (size_t)(h * 128 + (t + 1) * 32 + r) * 512 + c * 8);
        }
      }
      __syncthreads();
#pragma unroll
      for (int j = 0; j < 8; ++j) {
        int r0 = 8 * g + q4;
        s16x4 lo = __builtin_amdgcn_ds_read_tr16_b64_v4i16((LAS s16x4*)(st + w * 8192 + swz(r0, 2 * j + (p4 >> 1)) + 8 * (p4 & 1)));
        s16x4 hi = __builtin_amdgcn_ds_read_tr16_b64_v4i16((LAS s16x4*)(st + w * 8192 + swz(r0 + 4, 2 * j + (p4 >> 1)) + 8 * (p4 & 1)));
        s16x8 tt = __builtin_shufflevector(lo, hi, 0, 1, 2, 3, 4, 5, 6, 7);
        *(s16x8*)(smem + L_WO + ((w * 8 + j) * 4 + t) * 1024 + l * 16) = tt;
      }
      __syncthreads();
    }
    if (l == 0) { wait1(fl + F_TILE + h, 1u); wait1(fl + F_TILE + 32 + h, 1u); }
    asm volatile("" ::: "memory");
#pragma unroll
    for (int i = 0; i < 2; ++i) {
      int ch = tid + i * 256, r = ch >> 4, c = ch & 15;
      unsigned off = (unsigned)(r * NQKV + 4096 + h * 128 + c * 8) * 2;
      *(u32x4*)(smem + L_K + swz(8 + r, c)) = __builtin_amdgcn_raw_buffer_load_b128(wr, O_QKV + off, 0, 16);
      *(u32x4*)(smem + L_V + swz(8 + r, c)) = __builtin_amdgcn_raw_buffer_load_b128(wr, O_QKV + off + 8192, 0, 16);
    }
  }

  const float scale = 0.08838834764831845f;
  unsigned aQ[4], aK[4], aV[2], aE[2], aM, aW;
  {
    const int tid = threadIdx.x, w = tid >> 6, l = tid & 63, g = l >> 4, c16 = l & 15, q4 = c16 >> 2, p4 = c16 & 3;
#pragma unroll
    for (int t = 0; t < 4; ++t) { aQ[t] = L_Q + swz(c16 & 7, 4 * t + g); aK[t] = L_K + swz(c16, 4 * t + g); }
#pragma unroll
    for (int c = 0; c < 2; ++c) {
      aV[c] = L_V + swz(4 * g + q4, 2 * (2 * w + c) + (p4 >> 1)) + 8 * (p4 & 1);
      aE[c] = 256u * c16 + 16u * ((4 * w + 2 * c + (g >> 1)) ^ (((c16 & 3) << 2) | ((c16 >> 2) & 3))) + (g & 1) * 8;
    }
    aM = L_MEM + (c16 & 7) * 1088 + g * 16;
    aW = L_WO + w * 32768 + l * 16;
  }

  for (int s = 0; s < NSEG; ++s) {
    int tid = threadIdx.x;
    asm volatile("" : "+v"(tid));
    const int w = tid >> 6, l = tid & 63, g = l >> 4, c16 = l & 15;
    const int q4 = c16 >> 2, p4 = c16 & 3;
    unsigned spins = 0;
    STAMP(0)
    if (s == 0) {
#pragma unroll
      for (int i = 0; i < 2; ++i) {
        int ch = tid + i * 256, r = ch >> 6, c = ch & 63;
        u32x4 v = *(const u32x4*)(mem0 + r * 512 + c * 8);
        *(u32x4*)(smem + L_MEM + r * 1088 + c * 16) = v;
      }
    } else {
      u32x4 a[4];
      while (true) {
        unsigned ok = 1u;
        asm volatile("" ::: "memory");
#pragma unroll
        for (int i = 0; i < 4; ++i) a[i] = __builtin_amdgcn_raw_buffer_load_b128(wr, O_P2 + (unsigned)(s * 4096) * 4 + (i * 256 + tid) * 16, 0, 16);
#pragma unroll
        for (int i = 0; i < 4; ++i) ok &= a[i][0] & a[i][1] & a[i][2] & a[i][3];
        if (((s + 1) & 3) == 0 && s + 1 < NSEG) {
          const unsigned fo = O_FL + (unsigned)(F_TILE + ((s + 1) >> 2) * 64 + h) * 4;
          ok &= __builtin_amdgcn_raw_buffer_load_b32(wr, fo, 0, 16) & __builtin_amdgcn_raw_buffer_load_b32(wr, fo + 128, 0, 16);
        }
        if (__all(ok & 1u) || ++spins > (1u << 20)) break;
      }
#pragma unroll
      for (int i = 0; i < 4; ++i) {
        int ch = i * 256 + tid, r = ch >> 5, qy = (ch >> 2) & 7, qd = ch & 3;
        u32x2 v; v[0] = pack2(__uint_as_float(a[i][0]), __uint_as_float(a[i][1])); v[1] = pack2(__uint_as_float(a[i][2]), __uint_as_float(a[i][3]));
        *(u32x2*)(smem + L_MEM + qy * 1088 + (16 * r + 4 * qd) * 2) = v;
      }
    }
    FS(0)
    __syncthreads();
    FS(1)
    if (s > 0 && threadIdx.x == 0) __hip_atomic_fetch_add(fl + F_M + (s - 1) * 16, 1u, RLX_AGENT);
    STAMP(1)
    f32x4 acc[3][2];
#pragma unroll
    for (int m = 0; m < 3; ++m)
#pragma unroll
      for (int c = 0; c < 2; ++c) acc[m][c] = (f32x4){0, 0, 0, 0};
    {
      const char* mb = smem + aM;
      bf16x8 mf[16];
#pragma unroll
      for (int t = 0; t < 3; ++t) mf[t] = *(const bf16x8*)(mb + t * 64);
#pragma unroll
      for (int t = 0; t < 16; ++t) {
        if (t + 3 < 16) mf[t + 3] = *(const bf16x8*)(mb + (t + 3) * 64);
#pragma unroll
        for (int m = 0; m < 3; ++m)
#pragma unroll
          for (int c = 0; c < 2; ++c) acc[m][c] = __builtin_amdgcn_mfma_f32_16x16x32_bf16(wf[m][c][t], mf[t], acc[m][c], 0, 0, 0);
        __builtin_amdgcn_sched_barrier(0);
      }
    }
    FS(2)
    if (c16 < 8) {
#pragma unroll
      for (int c = 0; c < 2; ++c) {
        const unsigned off = aE[c];
        u32x2 vq, vk, vv;
        vq[0] = pack2(acc[0][c][0], acc[0][c][1]); vq[1] = pack2(acc[0][c][2], acc[0][c][3]);
        vk[0] = pack2(acc[1][c][0], acc[1][c][1]); vk[1] = pack2(acc[1][c][2], acc[1][c][3]);
        vv[0] = pack2(acc[2][c][0], acc[2][c][1]); vv[1] = pack2(acc[2][c][2], acc[2][c][3]);
        *(u32x2*)(smem + L_Q + off) = vq;
        *(u32x2*)(smem + L_K + off) = vk;
        *(u32x2*)(smem + L_V + off) = vv;
      }
    }
    FS(3)
    __syncthreads();
    FS(4)
    STAMP(2)
    f32x4 sc[3];
    s16x4 va[2][3];
    {
      bf16x8 qf[4], kf[3][4];
#pragma unroll
      for (int t = 0; t < 4; ++t) {
        qf[t] = *(const bf16x8*)(smem + aQ[t]);
        kf[0][t] = *(const bf16x8*)(smem + aK[t]);
      }
#pragma unroll
      for (int t = 0; t < 4; ++t) kf[1][t] = *(const bf16x8*)(smem + aK[t] + 4096);
      __builtin_amdgcn_sched_barrier(0);
      sc[0] = (f32x4){0, 0, 0, 0};
#pragma unroll
      for (int t = 0; t < 4; ++t) sc[0] = __builtin_amdgcn_mfma_f32_16x16x32_bf16(kf[0][t], qf[t], sc[0], 0, 0, 0);
      __builtin_amdgcn_sched_barrier(0);
#pragma unroll
      for (int t = 0; t < 4; ++t) kf[2][t] = *(const bf16x8*)(smem + aK[t] + 8192);
      sc[1] = (f32x4){0, 0, 0, 0};
#pragma unroll
      for (int t = 0; t < 4; ++t) sc[1] = __builtin_amdgcn_mfma_f32_16x16x32_bf16(kf[1][t], qf[t], sc[1], 0, 0, 0);
      __builtin_amdgcn_sched_barrier(0);
#pragma unroll
      for (int c = 0; c < 2; ++c) {
#pragma unroll
        for (int kt = 0; kt < 3; ++kt)
          va[c][kt] = __builtin_amdgcn_ds_read_tr16_b64_v4i16((LAS s16x4*)(smem + aV[c] + 4096 * kt));
      }
      sc[2] = (f32x4){0, 0, 0, 0};
#pragma unroll
      for (int t = 0; t < 4; ++t) sc[2] = __builtin_amdgcn_mfma_f32_16x16x32_bf16(kf[2][t], qf[t], sc[2], 0, 0, 0);
    }
    __builtin_amdgcn_sched_barrier(0);
    FS(5)
    bf16x8 pf0, pf1;
    {
      const bool hi = c16 >= 8;
      float xv[6];
#pragma unroll
      for (int r = 0; r < 4; ++r) xv[r] = hi ? sc[1][r] : sc[0][r];
      xv[4] = hi ? sc[2][2] : sc[2][0];
      xv[5] = hi ? sc[2][3] : sc[2][1];
      float mx = -3.0e38f;
#pragma unroll
      for (int r = 0; r < 6; ++r) {
        float v = rbf(rbf(xv[r]) * scale);
        if (r >= 4 && g >= 2) v = -3.0e38f;
        xv[r] = v;
        mx = fmaxf(mx, v);
      }
      mx = fmaxf(mx, __uint_as_float(__builtin_amdgcn_update_dpp(0, __float_as_uint(mx), 0x128, 0xf, 0xf, false)));
      {
        u32x2 r = __builtin_amdgcn_permlane16_swap(__float_as_uint(mx), __float_as_uint(mx), false, false);
        mx = fmaxf(__uint_as_float(r[0]), __uint_as_float(r[1]));
        r = __builtin_amdgcn_permlane32_swap(__float_as_uint(mx), __float_as_uint(mx), false, false);
        mx = fmaxf(__uint_as_float(r[0]), __uint_as_float(r[1]));
      }
      const float mxl = mx * 1.4426950408889634f;
      float sum = 0.f;
#pragma unroll
      for (int r = 0; r < 6; ++r) {
        float e = __builtin_amdgcn_exp2f(__builtin_fmaf(xv[r], 1.4426950408889634f, -mxl));
        if (r >= 4 && g >= 2) e = 0.f;
        xv[r] = e;
        sum += e;
      }
      sum += __uint_as_float(__builtin_amdgcn_update_dpp(0, __float_as_uint(sum), 0x128, 0xf, 0xf, false));
      {
        u32x2 r = __builtin_amdgcn_permlane16_swap(__float_as_uint(sum), __float_as_uint(sum), false, false);
        sum = __uint_as_float(r[0]) + __uint_as_float(r[1]);
        r = __builtin_amdgcn_permlane32_swap(__float_as_uint(sum), __float_as_uint(sum), false, false);
        sum = __uint_as_float(r[0]) + __uint_as_float(r[1]);
      }
      const float inv = __builtin_amdgcn_rcpf(sum);
      unsigned k0 = pack2(xv[0] * inv, xv[1] * inv), k1 = pack2(xv[2] * inv, xv[3] * inv), k2 = pack2(xv[4] * inv, xv[5] * inv);
      unsigned o0 = __builtin_amdgcn_update_dpp(0, k0, 0x128, 0xf, 0xf, false);
      unsigned o1 = __builtin_amdgcn_update_dpp(0, k1, 0x128, 0xf, 0xf, false);
      unsigned o2 = __builtin_amdgcn_update_dpp(0, k2, 0x128, 0xf, 0xf, false);
      u32x4 pb0, pb1;
      pb0[0] = k0; pb0[1] = k1; pb0[2] = o0; pb0[3] = o1;
      pb1[0] = k2; pb1[1] = o2; pb1[2] = 0; pb1[3] = 0;
      pf0 = __builtin_bit_cast(bf16x8, pb0); pf1 = __builtin_bit_cast(bf16x8, pb1);
    }
    FS(6)
    {
      f32x4 o[2];
#pragma unroll
      for (int c = 0; c < 2; ++c) {
        s16x8 v0 = __builtin_shufflevector(va[c][0], va[c][1], 0, 1, 2, 3, 4, 5, 6, 7);
        o[c] = __builtin_amdgcn_mfma_f32_16x16x32_bf16(__builtin_bit_cast(bf16x8, v0), pf0, (f32x4){0, 0, 0, 0}, 0, 0, 0);
      }
#pragma unroll
      for (int c = 0; c < 2; ++c) {
        s16x8 v1 = __builtin_shufflevector(va[c][2], va[c][2], 0, 1, 2, 3, 4, 5, 6, 7);
        o[c] = __builtin_amdgcn_mfma_f32_16x16x32_bf16(__builtin_bit_cast(bf16x8, v1), pf1, o[c], 0, 0, 0);
      }
      if (c16 < 8) {
#pragma unroll
        for (int c = 0; c < 2; ++c) {
          u32x2 v; v[0] = pack2(o[c][0], o[c][1]); v[1] = pack2(o[c][2], o[c][3]);
          *(u32x2*)(smem + L_MEM + aE[c]) = v;
        }
      }
    }
    FS(7)
    __syncthreads();
    FS(8)
    STAMP(3)
    if (s + 1 < NSEG) {
      bf16x8 of[4];
#pragma unroll
      for (int t = 0; t < 4; ++t) of[t] = *(const bf16x8*)(smem + aQ[t] + (L_MEM - L_Q));
      const char* wb = smem + aW;
      bf16x8 bw[32];
      constexpr int PD = 8;
      const unsigned base = O_P1 + (unsigned)((((s + 1) * 32 + 8 * w + (c16 >> 3)) * 32 + h) * 128 + (c16 & 7) * 16 + 4 * g) * 4;
#pragma unroll
      for (int u = 0; u < PD; ++u) bw[u] = *(const bf16x8*)(wb + u * 1024);
      f32x4 de, dod;
#pragma unroll
      for (int jp = 0; jp <= 4; ++jp) {
        f32x4 d0, d1;
        if (jp < 4) {
#pragma unroll
          for (int t = 0; t < 8; ++t) {
            const int u = jp * 8 + t;
            if (u + PD < 32) bw[u + PD] = *(const bf16x8*)(wb + (u + PD) * 1024);
            if (t == 0) d0 = __builtin_amdgcn_mfma_f32_16x16x32_bf16(bw[u], of[0], (f32x4){0, 0, 0, 0}, 0, 0, 0);
            else if (t < 4) d0 = __builtin_amdgcn_mfma_f32_16x16x32_bf16(bw[u], of[t], d0, 0, 0, 0);
            else if (t == 4) d1 = __builtin_amdgcn_mfma_f32_16x16x32_bf16(bw[u], of[0], (f32x4){0, 0, 0, 0}, 0, 0, 0);
            else d1 = __builtin_amdgcn_mfma_f32_16x16x32_bf16(bw[u], of[t - 4], d1, 0, 0, 0);
          }
        }
        if (jp > 0) {
          u32x4 v;
#pragma unroll
          for (int r = 0; r < 4; ++r)
            v[r] = 1u | (unsigned)__builtin_amdgcn_update_dpp(__float_as_uint(de[r]), __float_as_uint(dod[r]), 0x128, 0xf, 0xc, false);
          __builtin_amdgcn_raw_buffer_store_b128(v, wr, base + (jp - 1) * (2 * 32 * 128 * 4), 0, 16);
        }
        de = d0; dod = d1;
        __builtin_amdgcn_sched_barrier(0);
      }
      FS(9)
      FS(10)
      STAMP(4)
      {
        int r = tid >> 5, c = tid & 31;
        u32x4 v = *(const u32x4*)(smem + (c < 16 ? L_K : L_V) + swz(r, c & 15));
        __builtin_amdgcn_raw_buffer_store_b128(v, wr, O_KVM + (unsigned)((s * MM + r) * 8192 + (c >> 4) * 4096 + h * 128 + (c & 15) * 8) * 2, 0, 16);
      }
      f32x4 t4;
      while (true) {
        u32x4 a[4]; unsigned ok = 1u;
        asm volatile("" ::: "memory");
#pragma unroll
        for (int i = 0; i < 4; ++i)
          a[i] = __builtin_amdgcn_raw_buffer_load_b128(wr, O_P1 + (unsigned)((((s + 1) * 32 + h) * 32 + i * 8 + (l >> 3)) * 128 + 32 * w + (l & 7) * 4) * 4, 0, 16);
        t4 = (f32x4){0, 0, 0, 0};
#pragma unroll
        for (int i = 0; i < 4; ++i) {
          ok &= a[i][0] & a[i][1] & a[i][2] & a[i][3];
#pragma unroll
          for (int r = 0; r < 4; ++r) t4[r] += __uint_as_float(a[i][r]);
        }
        if (__all(ok & 1u) || ++spins > (1u << 20)) break;
      }
      FS(11)
      STAMP(5)
      u32x4 v;
#pragma unroll
      for (int r = 0; r < 4; ++r) {
        float x = t4[r];
        x += __uint_as_float(__builtin_amdgcn_update_dpp(0, __float_as_uint(x), 0x128  , 0xf, 0xf, false));
        u32x2 q = __builtin_amdgcn_permlane16_swap(__float_as_uint(x), __float_as_uint(x), false, false);
        x = __uint_as_float(q[0]) + __uint_as_float(q[1]);
        q = __builtin_amdgcn_permlane32_swap(__float_as_uint(x), __float_as_uint(x), false, false);
        x = __uint_as_float(q[0]) + __uint_as_float(q[1]);
        v[r] = __float_as_uint(x) | 1u;
      }
      if (l < 8) __builtin_amdgcn_raw_buffer_store_b128(v, wr, O_P2 + (unsigned)((s + 1) * 4096 + h * 128 + 32 * w + l * 4) * 4, 0, 16);
      {
        const int wu = __builtin_amdgcn_readfirstlane(w);
#pragma unroll
        for (int i = 0; i < 2; ++i) {
          const int r4 = wu * 8 + i * 4;
          const int rr = r4 + (l >> 4), row = 8 + rr;
          const int cc = (l & 15) ^ (((row & 3) << 2) | ((row >> 2) & 3));
          const char* src = (const char*)p.qkv + ((size_t)((s + 1) * SEG + rr) * NQKV + 4096 + h * 128 + cc * 8) * 2;
          __builtin_amdgcn_global_load_lds((const void*)src, (LAS void*)(smem + L_K + (8 + r4) * 256), 16, 0, 16);
          __builtin_amdgcn_global_load_lds((const void*)(src + 8192), (LAS void*)(smem + L_V + (8 + r4) * 256), 16, 0, 16);
        }
      }
      FS(12)
      STAMP(6)
    } else {
      int r = tid >> 5, c = tid & 31;
      u32x4 v = *(const u32x4*)(smem + (c < 16 ? L_K : L_V) + swz(r, c & 15));
      __builtin_amdgcn_raw_buffer_store_b128(v, wr, O_KVM + (unsigned)((s * MM + r) * 8192 + (c >> 4) * 4096 + h * 128 + (c & 15) * 8) * 2, 0, 16);
      asm volatile("s_waitcnt vmcnt(0)" ::: "memory");
      __syncthreads();
      if (threadIdx.x == 0) __hip_atomic_fetch_add(fl + F_M + s * 16, 1u, RLX_AGENT);
    }
  }
}

__device__ __forceinline__ void task_attn(const Params& p, char* smem, int s, int hg) {
  const int tid = threadIdx.x, w = tid >> 6, l = tid & 63, g = l >> 4, c16 = l & 15;
  const int q4 = c16 >> 2, p4 = c16 & 3;
  typedef __attribute__((ext_vector_type(8))) short s16x8;
  const u16* __restrict__ qkv = p.qkv;
  const u16* __restrict__ kvm = p.kvm;
  const float scale = 0.08838834764831845f;
  char* sv = smem + w * 48 * 256;
  const int h = hg * 4 + w;
  u32x4 vst[12];
#pragma unroll
  for (int i = 0; i < 12; ++i) {
    int ch = l + i * 64, r = ch >> 4, c = ch & 15;
    u32x4 v = (u32x4){0, 0, 0, 0};
    if (r < 8) v = *(const u32x4*)(kvm + (size_t)(s * MM + r) * 8192 + 4096 + h * 128 + c * 8);
    else if (r < 40) v = *(const u32x4*)(qkv + (size_t)(s * SEG + r - 8) * NQKV + 8192 + h * 128 + c * 8);
    vst[i] = v;
  }
  bf16x8 qf[4][2], kf[4][3];
#pragma unroll
  for (int t = 0; t < 4; ++t) {
#pragma unroll
    for (int qt = 0; qt < 2; ++qt)
      qf[t][qt] = *(const bf16x8*)(qkv + (size_t)(s * SEG + 16 * qt + c16) * NQKV + h * 128 + 32 * t + 8 * g);
#pragma unroll
    for (int kt = 0; kt < 3; ++kt) {
      int key = 16 * kt + c16;
      const u16* src = key < 8 ? kvm + (size_t)(s * MM + key) * 8192 + h * 128
                               : qkv + (size_t)(s * SEG + (key < 40 ? key - 8 : 0)) * NQKV + 4096 + h * 128;
      kf[t][kt] = *(const bf16x8*)(src + 32 * t + 8 * g);
    }
  }
#pragma unroll
  for (int i = 0; i < 12; ++i) {
    int ch = l + i * 64, r = ch >> 4, c = ch & 15;
    *(u32x4*)(sv + swz(r, c)) = vst[i];
  }
  f32x4 sc[3][2];
#pragma unroll
  for (int kt = 0; kt < 3; ++kt)
#pragma unroll
    for (int qt = 0; qt < 2; ++qt) sc[kt][qt] = (f32x4){0, 0, 0, 0};
#pragma unroll
  for (int t = 0; t < 4; ++t)
#pragma unroll
    for (int kt = 0; kt < 3; ++kt)
#pragma unroll
      for (int qt = 0; qt < 2; ++qt) sc[kt][qt] = __builtin_amdgcn_mfma_f32_16x16x32_bf16(kf[t][kt], qf[t][qt], sc[kt][qt], 0, 0, 0);
  bf16x8 pf0[2], pf1[2];
#pragma unroll
  for (int qt = 0; qt < 2; ++qt) {
    int qi = 8 + 16 * qt + c16;
    float mx = -3.0e38f;
#pragma unroll
    for (int kt = 0; kt < 3; ++kt)
#pragma unroll
      for (int r = 0; r < 4; ++r) {
        int key = 16 * kt + 4 * g + r;
        float v = rbf(rbf(sc[kt][qt][r]) * scale);
        if (key > qi) v = -1e9f;
        if (key >= 40) v = -3.0e38f;
        sc[kt][qt][r] = v;
        mx = fmaxf(mx, v);
      }
    mx = fmaxf(mx, __shfl_xor(mx, 16));
    mx = fmaxf(mx, __shfl_xor(mx, 32));
    float sum = 0.f;
#pragma unroll
    for (int kt = 0; kt < 3; ++kt)
#pragma unroll
      for (int r = 0; r < 4; ++r) {
        int key = 16 * kt + 4 * g + r;
        float e = (key >= 40) ? 0.f : __expf(sc[kt][qt][r] - mx);
        sc[kt][qt][r] = e;
        sum += e;
      }
    sum += __shfl_xor(sum, 16);
    sum += __shfl_xor(sum, 32);
    float inv = 1.f / sum;
    u32x4 b0, b1;
    b0[0] = pack2(sc[0][qt][0] * inv, sc[0][qt][1] * inv); b0[1] = pack2(sc[0][qt][2] * inv, sc[0][qt][3] * inv);
    b0[2] = pack2(sc[1][qt][0] * inv, sc[1][qt][1] * inv); b0[3] = pack2(sc[1][qt][2] * inv, sc[1][qt][3] * inv);
    b1[0] = pack2(sc[2][qt][0] * inv, sc[2][qt][1] * inv); b1[1] = pack2(sc[2][qt][2] * inv, sc[2][qt][3] * inv);
    b1[2] = 0; b1[3] = 0;
    pf0[qt] = __builtin_bit_cast(bf16x8, b0);
    pf1[qt] = __builtin_bit_cast(bf16x8, b1);
  }
  __builtin_amdgcn_s_waitcnt(0xc07f);
  __builtin_amdgcn_wave_barrier();
#pragma unroll
  for (int c = 0; c < 8; ++c) {
    int ch = 2 * c + (p4 >> 1);
    s16x4 a0 = __builtin_amdgcn_ds_read_tr16_b64_v4i16((LAS s16x4*)(sv + swz(4 * g + q4, ch) + 8 * (p4 & 1)));
    s16x4 a1 = __builtin_amdgcn_ds_read_tr16_b64_v4i16((LAS s16x4*)(sv + swz(16 + 4 * g + q4, ch) + 8 * (p4 & 1)));
    s16x4 a2 = __builtin_amdgcn_ds_read_tr16_b64_v4i16((LAS s16x4*)(sv + swz(32 + 4 * g + q4, ch) + 8 * (p4 & 1)));
    s16x8 v0 = __builtin_shufflevector(a0, a1, 0, 1, 2, 3, 4, 5, 6, 7);
    s16x8 v1 = __builtin_shufflevector(a2, a2, 0, 1, 2, 3, 4, 5, 6, 7);
#pragma unroll
    for (int qt = 0; qt < 2; ++qt) {
      f32x4 o = (f32x4){0, 0, 0, 0};
      o = __builtin_amdgcn_mfma_f32_16x16x32_bf16(__builtin_bit_cast(bf16x8, v0), pf0[qt], o, 0, 0, 0);
      o = __builtin_amdgcn_mfma_f32_16x16x32_bf16(__builtin_bit_cast(bf16x8, v1), pf1[qt], o, 0, 0, 0);
      u32x2 v; v[0] = pack2(o[0], o[1]); v[1] = pack2(o[2], o[3]);
      st8_sc1(p.ox + (size_t)(s * SEG + 16 * qt + c16) * 4096 + h * 128 + 16 * c + 4 * g, v);
    }
  }
}

static constexpr int E_WO = 0;
static constexpr int E_RED = 131072;
__device__ __forceinline__ void load_wo_slice(const Params& p, char* smem, int cb) {
  const int tid = threadIdx.x, w = tid >> 6, l = tid & 63, g = l >> 4, c16 = l & 15;
  const int q4 = c16 >> 2, p4 = c16 & 3;
  typedef __attribute__((ext_vector_type(8))) short s16x8;
  char* st = smem + E_RED;
  for (int r8 = 0; r8 < 8; ++r8) {
    u32x4 v[4];
#pragma unroll
    for (int i = 0; i < 4; ++i) {
      int ch = tid + i * 256, r = ch >> 1, c = ch & 1;
      v[i] = *(const u32x4*)(p.Wo + (size_t)(r8 * 512 + r) * DM + cb * 16 + c * 8);
    }
#pragma unroll
    for (int i = 0; i < 4; ++i) {
      int ch = tid + i * 256, r = ch >> 1, c = ch & 1;
      *(u32x4*)(st + r * 32 + c * 16) = v[i];
    }
    __syncthreads();
#pragma unroll
    for (int t = 0; t < 4; ++t) {
      int ks = 4 * w + t;
      int r0 = 32 * ks + 8 * g + q4;
      s16x4 lo = __builtin_amdgcn_ds_read_tr16_b64_v4i16((LAS s16x4*)(st + r0 * 32 + p4 * 8));
      s16x4 hi = __builtin_amdgcn_ds_read_tr16_b64_v4i16((LAS s16x4*)(st + (r0 + 4) * 32 + p4 * 8));
      s16x8 tt = __builtin_shufflevector(lo, hi, 0, 1, 2, 3, 4, 5, 6, 7);
      *(s16x8*)(smem + E_WO + (r8 * 16 + ks) * 1024 + l * 16) = tt;
    }
    __syncthreads();
  }
}
__device__ __forceinline__ void task_out(const Params& p, char* smem, int s, int cb) {
  const int tid = threadIdx.x, w = tid >> 6, l = tid & 63, g = l >> 4, c16 = l & 15;
  f32x4 acc[2];
  acc[0] = (f32x4){0, 0, 0, 0}; acc[1] = (f32x4){0, 0, 0, 0};
  const u16* a0 = p.ox + (size_t)(s * SEG + c16) * 4096 + w * 1024 + 8 * g;
  const u16* a1 = a0 + 16 * 4096;
  const char* wb = smem + E_WO + w * 32768 + l * 16;
#pragma unroll
  for (int hb = 0; hb < 2; ++hb) {
    bf16x8 x0[16], x1[16];
#pragma unroll
    for (int t = 0; t < 16; ++t) { x0[t] = *(const bf16x8*)(a0 + 32 * (hb * 16 + t)); x1[t] = *(const bf16x8*)(a1 + 32 * (hb * 16 + t)); }
    __builtin_amdgcn_sched_barrier(0);
#pragma unroll
    for (int t = 0; t < 16; ++t) {
      bf16x8 wf = *(const bf16x8*)(wb + (hb * 16 + t) * 1024);
      acc[0] = __builtin_amdgcn_mfma_f32_16x16x32_bf16(wf, x0[t], acc[0], 0, 0, 0);
      acc[1] = __builtin_amdgcn_mfma_f32_16x16x32_bf16(wf, x1[t], acc[1], 0, 0, 0);
    }
    __builtin_amdgcn_sched_barrier(0);
  }
  *(f32x4*)(smem + E_RED + (w * 2 + 0) * 1024 + l * 16) = acc[0];
  *(f32x4*)(smem + E_RED + (w * 2 + 1) * 1024 + l * 16) = acc[1];
  __syncthreads();
  if (w < 2) {
    f32x4 a = *(f32x4*)(smem + E_RED + w * 1024 + l * 16);
#pragma unroll
    for (int i = 1; i < 4; ++i) a += *(f32x4*)(smem + E_RED + (i * 2 + w) * 1024 + l * 16);
    int m = s * SEG + 16 * w + c16, n = cb * 16 + 4 * g;
    u32x2 gg = *(const u32x2*)(p.g + (size_t)m * DM + n), xx = *(const u32x2*)(p.x + (size_t)m * DM + n);
    float gf[4] = {bf2f(gg[0] & 0xffff), bf2f(gg[0] >> 16), bf2f(gg[1] & 0xffff), bf2f(gg[1] >> 16)};
    float xf[4] = {bf2f(xx[0] & 0xffff), bf2f(xx[0] >> 16), bf2f(xx[1] & 0xffff), bf2f(xx[1] >> 16)};
    float r[4];
#pragma unroll
    for (int i = 0; i < 4; ++i) r[i] = xf[i] + rbf(rbf(a[i]) * gf[i]);
    u32x2 o; o[0] = pack2(r[0], r[1]); o[1] = pack2(r[2], r[3]);
    *(u32x2*)(p.out + (size_t)m * DM + n) = o;
  }
  __syncthreads();
}

__device__ __forceinline__ void worker(const Params& p, char* smem) {
  constexpr int NWK = 224;
  const int wid = blockIdx.x - NH;
  for (int t = wid; t < 1024; t += NWK) {
    int rb = t >> 6, ct = t & 63;
    const u16* B = ct < 32 ? p.Wk : p.Wv;
    EpiStore epi{p.qkv + 4096 + (ct >> 5) * 4096, NQKV, 0};
    gemm_tile<128, 128, 64, EpiStore>(smem, p.xn, DM, B, 4096, S_, 4096, DM, rb * 128, (ct & 31) * 128, epi);
    publish(p.fl + F_TILE + t, p.fl + F_QKV + rb * 16);
  }
  for (int t = (wid + 224 - (1024 % NWK)) % NWK; t < 176; t += NWK) {
    int rb = t / 11, ct = t % 11;
    EpiGelu epi{p.h1, FF, 0, p.b1};
    gemm_tile<128, 128, 64, EpiGelu>(smem, p.y, DM, p.w1, FF, S_, FF, DM, rb * 128, ct * 128, epi);
    publish(p.fl + F_H + rb * 16, nullptr);
  }
  for (int t = (wid + 48) % NWK; t < 512; t += NWK) {
    int rb = t >> 5, ct = t & 31;
    EpiStore epi{p.qkv, NQKV, 0};
    gemm_tile<128, 128, 64, EpiStore>(smem, p.xn, DM, p.Wq, 4096, S_, 4096, DM, rb * 128, ct * 128, epi);
    publish(p.fl + F_QKV + rb * 16, nullptr);
  }
  for (int t = (wid + 120) % NWK; t < 64; t += NWK) {
    int rb = t >> 2, ct = t & 3;
    if (threadIdx.x == 0) wait1<64>(p.fl + F_H + rb * 16, 11u);
    acquire_all();
    EpiSilu epi{p.g, DM, 0, p.b2};
    gemm_tile<128, 128, 64, EpiSilu>(smem, p.h1, FF, p.w2, DM, S_, DM, FF, rb * 128, ct * 128, epi);
    publish(p.fl + F_G + rb * 16, nullptr);
  }
  if (wid < 32) {
    for (int s = wid >> 3; s < NSEG; s += 4) {
      if (threadIdx.x == 0) {
        wait1<64>(p.fl + F_QKV + (s >> 2) * 16, 96u);
        if (s >= 60) wait1<8>(p.fl + F_M + s * 16, 32u); else wait1<64>(p.fl + F_M + s * 16, 32u);
      }
      acquire_all();
      task_attn(p, smem, s, wid & 7);
      asm volatile("s_waitcnt vmcnt(0)" ::: "memory");
      __syncthreads();
      if (threadIdx.x == 0) {
        unsigned old = __hip_atomic_fetch_add(p.fl + F_OX + s * 16, 1u, RLX_AGENT);
        if (old == 7u)
          for (int c = 0; c < 32; ++c) __hip_atomic_store(p.fl + F_OXB + (s * 32 + c) * 16, 1u, RLX_AGENT);
      }
    }
  } else {
    const int j = wid - 32, cb = j & 31, grp = j >> 5;
    load_wo_slice(p, smem, cb);
    for (int s = grp; s < NSEG; s += 6) {
      if (threadIdx.x == 0) {
        wait1<64>(p.fl + F_G + (s >> 2) * 16, 4u);
        if (s >= 60) wait1<8>(p.fl + F_OXB + (s * 32 + cb) * 16, 1u); else wait1<64>(p.fl + F_OXB + (s * 32 + cb) * 16, 1u);
      }
      acquire_all();
      task_out(p, smem, s, cb);
    }
  }
}

__global__ __launch_bounds__(256, 1) void k_main(Params p) {
  extern __shared__ __attribute__((aligned(16))) char smem[];
  if (blockIdx.x < NH) recurrence(p, smem);
  else worker(p, smem);
}

extern "C" void kernel_launch(void* const* d_in, const int* in_sizes, int n_in, void* d_out, int out_size, void* d_ws,
                              size_t ws_size, hipStream_t stream) {
  Params p;
  p.x = (const u16*)d_in[0];
  p.mem0 = (const u16*)d_in[1];
  p.Wq = (const u16*)d_in[2];
  p.Wk = (const u16*)d_in[3];
  p.Wv = (const u16*)d_in[4];
  p.Wo = (const u16*)d_in[5];
  p.w1 = (const u16*)d_in[8];
  p.b1 = (const u16*)d_in[9];
  p.w2 = (const u16*)d_in[10];
  p.b2 = (const u16*)d_in[11];
  p.out = (u16*)d_out;
  char* ws = (char*)d_ws;
  size_t o = 0;
  auto take = [&](size_t bytes) { char* q = ws + o; o += (bytes + 255) & ~(size_t)255; return q; };
  p.fl = (unsigned*)take(F_WORDS * 4);
  p.P2 = (float*)take((size_t)NSEG * 4096 * 4);
  p.P1 = (float*)take((size_t)NSEG * 32 * 32 * 128 * 4);
  const size_t zbytes = o;
  p.xn = (u16*)take((size_t)S_ * DM * 2);
  p.y = (u16*)take((size_t)S_ * DM * 2);
  p.qkv = (u16*)take((size_t)S_ * NQKV * 2);
  p.h1 = (u16*)take((size_t)S_ * FF * 2);
  p.g = (u16*)take((size_t)S_ * DM * 2);
  p.kvm = (u16*)take((size_t)NSEG * MM * 8192 * 2);
  p.ox = (u16*)take((size_t)S_ * 4096 * 2);
  p.stamps = (unsigned long long*)(ws + 200 * 1024 * 1024);

  k_prep<<<512, 256, 0, stream>>>(p.x, p.xn, p.y, (float*)ws, (int)(zbytes / 16));
  hipFuncSetAttribute((const void*)k_main, hipFuncAttributeMaxDynamicSharedMemorySize, 163840);
  k_main<<<256, 256, 163840, stream>>>(p);
}
```

```cpp
#include <hip/hip_runtime.h>
#include <hip/hip_bf16.h>
#include <stdint.h>

typedef __attribute__((ext_vector_type(8))) __bf16 bf16x8;
typedef __attribute__((ext_vector_type(4))) __bf16 bf16x4;
typedef __attribute__((ext_vector_type(4))) float f32x4;
typedef __attribute__((ext_vector_type(4))) short s16x4;
typedef __attribute__((ext_vector_type(4))) unsigned u32x4;
typedef __attribute__((ext_vector_type(2))) unsigned u32x2;
typedef unsigned short u16;
#define LAS __attribute__((address_space(3)))

static constexpr int S_ = 2048, DM = 512, NH = 32, DH = 128, MM = 8, SEG = 32, NSEG = 64, FF = 1376;
static constexpr int NQKV = 3 * NH * DH;

__device__ __forceinline__ float bf2f(u16 v) { return __uint_as_float(((unsigned)v) << 16); }
__device__ __forceinline__ u16 f2bf(float f) {
  __bf16 b = (__bf16)f;
  return __builtin_bit_cast(u16, b);
}
__device__ __forceinline__ float rbf(float f) { return bf2f(f2bf(f)); }
typedef __attribute__((ext_vector_type(2))) float f32x2;
typedef __attribute__((ext_vector_type(2))) __bf16 bf16x2;
__device__ __forceinline__ unsigned pack2(float a, float b) {
  bf16x2 r = __builtin_convertvector((f32x2){a, b}, bf16x2);
  return __builtin_bit_cast(unsigned, r);
}

__global__ __launch_bounds__(256) void k_prep(const u16* __restrict__ x, u16* __restrict__ xn, u16* __restrict__ y,
                                              float* __restrict__ zb, int zn4) {
  int tid = threadIdx.x, w = tid >> 6, l = tid & 63;
  int row = blockIdx.x * 4 + w;
  {
    int gt = blockIdx.x * 256 + tid;
    for (int i = gt; i < zn4; i += 512 * 256) ((f32x4*)zb)[i] = (f32x4){0, 0, 0, 0};
  }
  u32x4 raw = *(const u32x4*)(x + (size_t)row * DM + l * 8);
  float v[8];
#pragma unroll
  for (int i = 0; i < 4; ++i) { v[2 * i] = bf2f(raw[i] & 0xffff); v[2 * i + 1] = bf2f(raw[i] >> 16); }
#pragma unroll
  for (int pass = 0; pass < 2; ++pass) {
    float s = 0.f;
#pragma unroll
    for (int i = 0; i < 8; ++i) s += v[i];
#pragma unroll
    for (int m = 32; m >= 1; m >>= 1) s += __shfl_xor(s, m);
    float mean = s * (1.f / DM);
    float q = 0.f;
#pragma unroll
    for (int i = 0; i < 8; ++i) { float d = v[i] - mean; q += d * d; }
#pragma unroll
    for (int m = 32; m >= 1; m >>= 1) q += __shfl_xor(q, m);
    float var = rbf(q * (1.f / DM));
    float mb = rbf(mean);
    float den = rbf(sqrtf(rbf(var + 1e-5f)));
#pragma unroll
    for (int i = 0; i < 8; ++i) v[i] = rbf(rbf(v[i] - mb) / den);
    u32x4 o;
#pragma unroll
    for (int i = 0; i < 4; ++i) o[i] = pack2(v[2 * i], v[2 * i + 1]);
    *(u32x4*)((pass == 0 ? xn : y) + (size_t)row * DM + l * 8) = o;
  }
}

struct Params {
  const u16 *x, *mem0, *Wq, *Wk, *Wv, *Wo, *w1, *b1, *w2, *b2;
  u16 *out, *xn, *y, *qkv, *h1, *g, *kvm, *ox;
  float *P1, *P2;
  unsigned* fl;
  unsigned long long* stamps;
};
#define RLX_AGENT __ATOMIC_RELAXED, __HIP_MEMORY_SCOPE_AGENT
static constexpr int F_TILE = 0;
static constexpr int F_QKV = 1024;
static constexpr int F_H = 1280;
static constexpr int F_G = 1536;
static constexpr int F_M = 2048;
static constexpr int F_Z = 1856;
static constexpr int F_OX = 3072;
static constexpr int F_OXB = 4096;
static constexpr int F_WORDS = 4096 + 64 * 32 * 16;
static constexpr unsigned O_FL = 0;
static constexpr unsigned O_P2 = F_WORDS * 4;
static constexpr unsigned O_P1 = O_P2 + NSEG * 4096 * 4;
static constexpr unsigned O_ZEND = O_P1 + NSEG * 32 * 32 * 128 * 4;
static constexpr unsigned O_XN = O_ZEND;
static constexpr unsigned O_Y = O_XN + S_ * DM * 2;
static constexpr unsigned O_QKV = O_Y + S_ * DM * 2;
static constexpr unsigned O_H1 = O_QKV + S_ * NQKV * 2;
static constexpr unsigned O_G = O_H1 + S_ * FF * 2;
static constexpr unsigned O_KVM = O_G + S_ * DM * 2;
static constexpr unsigned O_END = O_KVM + NSEG * MM * 8192 * 2;

__device__ __forceinline__ void st8_sc1(u16* p, u32x2 v) {
  __hip_atomic_store((unsigned long long*)p, (unsigned long long)v[0] | ((unsigned long long)v[1] << 32), RLX_AGENT);
}
__device__ __forceinline__ void publish(unsigned* c1, unsigned* c2) {
  asm volatile("s_waitcnt vmcnt(0)" ::: "memory");
  __syncthreads();
  if (threadIdx.x == 0) {
    __hip_atomic_fetch_add(c1, 1u, RLX_AGENT);
    if (c2) __hip_atomic_fetch_add(c2, 1u, RLX_AGENT);
  }
}
template <int SLP = 4>
__device__ __forceinline__ void wait1(unsigned* c, unsigned target) {
  unsigned spins = 0;
  while (__hip_atomic_load(c, RLX_AGENT) < target) {
    __builtin_amdgcn_s_sleep(SLP);
    if (++spins > (1u << 22)) break;
  }
}
__device__ __forceinline__ void acquire_all() {
  if (threadIdx.x == 0) {
    __builtin_amdgcn_fence(__ATOMIC_ACQUIRE, "agent");
    asm volatile("s_waitcnt vmcnt(0)" ::: "memory");
  }
  __syncthreads();
}

struct EpiStore {
  u16* C; int ldc; int pad_;
  __device__ __forceinline__ void operator()(int m, int n, f32x4 a) const {
    u32x2 o; o[0] = pack2(a[0], a[1]); o[1] = pack2(a[2], a[3]);
    st8_sc1(C + (size_t)m * ldc + n, o);
  }
};
struct EpiGelu {
  u16* C; int ldc; int pad_; const u16* b;
  __device__ __forceinline__ void operator()(int m, int n, f32x4 a) const {
    float r[4];
#pragma unroll
    for (int i = 0; i < 4; ++i) {
      float v = rbf(rbf(a[i]) + bf2f(b[n + i]));
      float u = 0.7978845608028654f * (v + 0.044715f * v * v * v);
      r[i] = 0.5f * v * (1.f + tanhf(u));
    }
    u32x2 o; o[0] = pack2(r[0], r[1]); o[1] = pack2(r[2], r[3]);
    st8_sc1(C + (size_t)m * ldc + n, o);
  }
};
struct EpiSilu {
  u16* C; int ldc; int pad_; const u16* b;
  __device__ __forceinline__ void operator()(int m, int n, f32x4 a) const {
    float r[4];
#pragma unroll
    for (int i = 0; i < 4; ++i) {
      float v = rbf(rbf(a[i]) + bf2f(b[n + i]));
      r[i] = v / (1.f + __expf(-v));
    }
    u32x2 o; o[0] = pack2(r[0], r[1]); o[1] = pack2(r[2], r[3]);
    st8_sc1(C + (size_t)m * ldc + n, o);
  }
};

template <int BM, int BN, int BK>
struct GemmCfg {
  static constexpr int ASTR = BK * 2 + 16;
  static constexpr int BSTR = BN * 2 + 32;
  static constexpr int ABYTES = BM * ASTR, BBYTES = BK * BSTR;
  static constexpr int LDS = 2 * (ABYTES + BBYTES);
};

template <int BM, int BN, int BK, class Epi>
__device__ __forceinline__ void gemm_tile(char* smem, const u16* __restrict__ A, int lda, const u16* __restrict__ B, int ldb,
                                          int M, int N, int K, int bm, int bn, const Epi& epi) {
  using C = GemmCfg<BM, BN, BK>;
  constexpr int WM = BM / 2, WN = BN / 2, TM = WM / 16, TN = WN / 16;
  constexpr int ACH = BM * BK / 8 / 256;
  constexpr int BCH = BK * BN / 8 / 256;
  static_assert(ACH >= 1 && BCH >= 1, "tile too small");
  const int tid = threadIdx.x, w = tid >> 6, l = tid & 63, g = l >> 4, c16 = l & 15;
  const int wm = w >> 1, wn = w & 1;
  f32x4 acc[TM][TN];
#pragma unroll
  for (int i = 0; i < TM; ++i)
#pragma unroll
    for (int j = 0; j < TN; ++j) acc[i][j] = (f32x4){0, 0, 0, 0};
  u32x4 ra[ACH], rb[BCH];
  auto gload = [&](int k0) {
#pragma unroll
    for (int i = 0; i < ACH; ++i) {
      int ch = tid + i * 256, r = ch / (BK / 8), c = ch % (BK / 8);
      int k = k0 + c * 8;
      ra[i] = (k < K) ? *(const u32x4*)(A + (size_t)(bm + r) * lda + k) : (u32x4){0, 0, 0, 0};
    }
#pragma unroll
    for (int i = 0; i < BCH; ++i) {
      int ch = tid + i * 256, r = ch / (BN / 8), c = ch % (BN / 8);
      int k = k0 + r, n = bn + c * 8;
      rb[i] = (k < K && n < N) ? *(const u32x4*)(B + (size_t)k * ldb + n) : (u32x4){0, 0, 0, 0};
    }
  };
  auto lstore = [&](int buf) {
    char* sa = smem + buf * (C::ABYTES + C::BBYTES);
    char* sb = sa + C::ABYTES;
#pragma unroll
    for (int i = 0; i < ACH; ++i) {
      int ch = tid + i * 256, r = ch / (BK / 8), c = ch % (BK / 8);
      *(u32x4*)(sa + r * C::ASTR + c * 16) = ra[i];
    }
#pragma unroll
    for (int i = 0; i < BCH; ++i) {
      int ch = tid + i * 256, r = ch / (BN / 8), c = ch % (BN / 8);
      *(u32x4*)(sb + r * C::BSTR + ((c ^ (((r >> 3) & 1) * (BN / 16))) * 16)) = rb[i];
    }
  };
  const int nk = (K + BK - 1) / BK;
  gload(0);
  lstore(0);
  __syncthreads();
  for (int kt = 0; kt < nk; ++kt) {
    int buf = kt & 1;
    if (kt + 1 < nk) gload((kt + 1) * BK);
    const char* sa = smem + buf * (C::ABYTES + C::BBYTES);
    const char* sb = sa + C::ABYTES;
#pragma unroll
    for (int ks = 0; ks < BK / 32; ++ks) {
      bf16x8 af[TM], bfr[TN];
#pragma unroll
      for (int i = 0; i < TM; ++i)
        af[i] = *(const bf16x8*)(sa + (wm * WM + i * 16 + c16) * C::ASTR + (ks * 32 + g * 8) * 2);
#pragma unroll
      for (int j = 0; j < TN; ++j) {
        int q = c16 >> 2, p = c16 & 3;
        int col = wn * WN + j * 16 + p * 4;
        s16x4 lo, hi;
        {
          int r = ks * 32 + g * 8 + q;
          lo = __builtin_amdgcn_ds_read_tr16_b64_v4i16((LAS s16x4*)(sb + r * C::BSTR + ((col * 2) ^ (((r >> 3) & 1) * BN))));
          r += 4;
          hi = __builtin_amdgcn_ds_read_tr16_b64_v4i16((LAS s16x4*)(sb + r * C::BSTR + ((col * 2) ^ (((r >> 3) & 1) * BN))));
        }
        typedef __attribute__((ext_vector_type(8))) short s16x8;
        s16x8 t = __builtin_shufflevector(lo, hi, 0, 1, 2, 3, 4, 5, 6, 7);
        bfr[j] = __builtin_bit_cast(bf16x8, t);
      }
#pragma unroll
      for (int i = 0; i < TM; ++i)
#pragma unroll
        for (int j = 0; j < TN; ++j) acc[i][j] = __builtin_amdgcn_mfma_f32_16x16x32_bf16(bfr[j], af[i], acc[i][j], 0, 0, 0);
    }
    if (kt + 1 < nk) lstore(buf ^ 1);
    __syncthreads();
  }
#pragma unroll
  for (int i = 0; i < TM; ++i)
#pragma unroll
    for (int j = 0; j < TN; ++j) {
      int m = bm + wm * WM + i * 16 + c16, n = bn + wn * WN + j * 16 + g * 4;
      if (n < N) epi(m, n, acc[i][j]);
    }
}

__device__ __forceinline__ unsigned swz(unsigned row, unsigned ch) {
  return 256u * row + 16u * (ch ^ (((row & 3) << 2) | ((row >> 2) & 3)));
}

static constexpr int L_WO = 0;
static constexpr int L_V = 131072;
static constexpr int L_K = L_V + 40 * 256;
static constexpr int L_Q = L_K + 40 * 256;
static constexpr int L_MEM = L_Q + 8 * 256;
static constexpr int L_END = L_MEM + 8704;
static_assert(L_END <= 163840, "lds");

#define FS(i)
#define STAMP(i)

__device__ __forceinline__ void recurrence(const Params& p, char* smem) {
  const u16* __restrict__ Wq = p.Wq; const u16* __restrict__ Wk = p.Wk; const u16* __restrict__ Wv = p.Wv;
  const u16* __restrict__ Wo = p.Wo; const u16* __restrict__ mem0 = p.mem0;
  unsigned long long* stamps = p.stamps;
  const int h = blockIdx.x;
  const __amdgpu_buffer_rsrc_t wr = __builtin_amdgcn_make_buffer_rsrc(p.fl, 0, O_END, 0x00020000);
  unsigned* const fl = p.fl;
  typedef __attribute__((ext_vector_type(8))) short s16x8;

  bf16x8 wf[3][2][16];
  {
    const int tid = threadIdx.x, w = tid >> 6, l = tid & 63, g = l >> 4, c16 = l & 15;
    const int q4 = c16 >> 2, p4 = c16 & 3;
#pragma unroll
    for (int m = 0; m < 3; ++m) {
      const u16* W = m == 0 ? Wq : (m == 1 ? Wk : Wv);
      {
        const int wu = __builtin_amdgcn_readfirstlane(w);
#pragma unroll
        for (int i = 0; i < 32; ++i) {
          const int r4 = (i * 4 + wu) * 4;
          const int row = r4 + (l >> 4);
          const int cc = (l & 15) ^ (((row & 3) << 2) | ((row >> 2) & 3));
          __builtin_amdgcn_global_load_lds((const void*)(W + (size_t)row * 4096 + h * 128 + cc * 8), (LAS void*)(smem + r4 * 256), 16, 0, 0);
        }
      }
      __syncthreads();
#pragma unroll
      for (int c = 0; c < 2; ++c)
#pragma unroll
        for (int t = 0; t < 16; ++t) {
          int ct = 2 * w + c;
          int r0 = 32 * t + 8 * g + q4;
          s16x4 lo = __builtin_amdgcn_ds_read_tr16_b64_v4i16((LAS s16x4*)(smem + swz(r0, 2 * ct + (p4 >> 1)) + 8 * (p4 & 1)));
          s16x4 hi = __builtin_amdgcn_ds_read_tr16_b64_v4i16((LAS s16x4*)(smem + swz(r0 + 4, 2 * ct + (p4 >> 1)) + 8 * (p4 & 1)));
          s16x8 tt = __builtin_shufflevector(lo, hi, 0, 1, 2, 3, 4, 5, 6, 7);
          wf[m][c][t] = __builtin_bit_cast(bf16x8, tt);
        }
      __syncthreads();
    }
    u32x4 v[8];
#pragma unroll
    for (int i = 0; i < 8; ++i) {
      int ch = tid + i * 256, r = ch >> 6, c = ch & 63;
      v[i] = *(const u32x4*)(Wo + (size_t)(h * 128 + r) * 512 + c * 8);
    }
    for (int t = 0; t < 4; ++t) {
      char* st = smem + 131072;
#pragma unroll
      for (int i = 0; i < 8; ++i) {
        int ch = tid + i * 256, r = ch >> 6, c = ch & 63;
        *(u32x4*)(st + (c >> 4) * 8192 + swz(r, c & 15)) = v[i];
      }
      if (t + 1 < 4) {
#pragma unroll
        for (int i = 0; i < 8; ++i) {
          int ch = tid + i * 256, r = ch >> 6, c = ch & 63;
          v[i] = *(const u32x4*)(Wo + (size_t)(h * 128 + (t + 1) * 32 + r) * 512 + c * 8);
        }
      }
      __syncthreads();
#pragma unroll
      for (int j = 0; j < 8; ++j) {
        int r0 = 8 * g + q4;
        s16x4 lo = __builtin_amdgcn_ds_read_tr16_b64_v4i16((LAS s16x4*)(st + w * 8192 + swz(r0, 2 * j + (p4 >> 1)) + 8 * (p4 & 1)));
        s16x4 hi = __builtin_amdgcn_ds_read_tr16_b64_v4i16((LAS s16x4*)(st + w * 8192 + swz(r0 + 4, 2 * j + (p4 >> 1)) + 8 * (p4 & 1)));
        s16x8 tt = __builtin_shufflevector(lo, hi, 0, 1, 2, 3, 4, 5, 6, 7);
        *(s16x8*)(smem + L_WO + ((w * 8 + j) * 4 + t) * 1024 + l * 16) = tt;
      }
      __syncthreads();
    }
    if (l == 0) { wait1(fl + F_Z, 224u); wait1(fl + F_TILE + h, 1u); wait1(fl + F_TILE + 32 + h, 1u); }
    asm volatile("" ::: "memory");
#pragma unroll
    for (int i = 0; i < 2; ++i) {
      int ch = tid + i * 256, r = ch >> 4, c = ch & 15;
      unsigned off = (unsigned)(r * NQKV + 4096 + h * 128 + c * 8) * 2;
      *(u32x4*)(smem + L_K + swz(8 + r, c)) = __builtin_amdgcn_raw_buffer_load_b128(wr, O_QKV + off, 0, 16);
      *(u32x4*)(smem + L_V + swz(8 + r, c)) = __builtin_amdgcn_raw_buffer_load_b128(wr, O_QKV + off + 8192, 0, 16);
    }
  }

  const float scale = 0.08838834764831845f;
  unsigned aQ[4], aK[4], aV[2], aE[2], aM, aW;
  {
    const int tid = threadIdx.x, w = tid >> 6, l = tid & 63, g = l >> 4, c16 = l & 15, q4 = c16 >> 2, p4 = c16 & 3;
#pragma unroll
    for (int t = 0; t < 4; ++t) { aQ[t] = L_Q + swz(c16 & 7, 4 * t + g); aK[t] = L_K + swz(c16, 4 * t + g); }
#pragma unroll
    for (int c = 0; c < 2; ++c) {
      aV[c] = L_V + swz(4 * g + q4, 2 * (2 * w + c) + (p4 >> 1)) + 8 * (p4 & 1);
      aE[c] = 256u * c16 + 16u * ((4 * w + 2 * c + (g >> 1)) ^ (((c16 & 3) << 2) | ((c16 >> 2) & 3))) + (g & 1) * 8;
    }
    aM = L_MEM + (c16 & 7) * 1088 + g * 16;
    aW = L_WO + w * 32768 + l * 16;
  }

  for (int s = 0; s < NSEG; ++s) {
    int tid = threadIdx.x;
    asm volatile("" : "+v"(tid));
    const int w = tid >> 6, l = tid & 63, g = l >> 4, c16 = l & 15;
    const int q4 = c16 >> 2, p4 = c16 & 3;
    unsigned spins = 0;
    STAMP(0)
    if (s == 0) {
#pragma unroll
      for (int i = 0; i < 2; ++i) {
        int ch = tid + i * 256, r = ch >> 6, c = ch & 63;
        u32x4 v = *(const u32x4*)(mem0 + r * 512 + c * 8);
        *(u32x4*)(smem + L_MEM + r * 1088 + c * 16) = v;
      }
    } else {
      u32x4 a[4];
      while (true) {
        unsigned ok = 1u;
        asm volatile("" ::: "memory");
#pragma unroll
        for (int i = 0; i < 4; ++i) a[i] = __builtin_amdgcn_raw_buffer_load_b128(wr, O_P2 + (unsigned)(s * 4096) * 4 + (i * 256 + tid) * 16, 0, 16);
#pragma unroll
        for (int i = 0; i < 4; ++i) ok &= a[i][0] & a[i][1] & a[i][2] & a[i][3];
        if (((s + 1) & 3) == 0 && s + 1 < NSEG) {
          const unsigned fo = O_FL + (unsigned)(F_TILE + ((s + 1) >> 2) * 64 + h) * 4;
          ok &= __builtin_amdgcn_raw_buffer_load_b32(wr, fo, 0, 16) & __builtin_amdgcn_raw_buffer_load_b32(wr, fo + 128, 0, 16);
        }
        if (__all(ok & 1u) || ++spins > (1u << 20)) break;
      }
#pragma unroll
      for (int i = 0; i < 4; ++i) {
        int ch = i * 256 + tid, r = ch >> 5, qy = (ch >> 2) & 7, qd = ch & 3;
        u32x2 v; v[0] = pack2(__uint_as_float(a[i][0]), __uint_as_float(a[i][1])); v[1] = pack2(__uint_as_float(a[i][2]), __uint_as_float(a[i][3]));
        *(u32x2*)(smem + L_MEM + qy * 1088 + (16 * r + 4 * qd) * 2) = v;
      }
    }
    FS(0)
    __syncthreads();
    FS(1)
    if (s > 0 && threadIdx.x == 0) __hip_atomic_fetch_add(fl + F_M + (s - 1) * 16, 1u, RLX_AGENT);
    STAMP(1)
    f32x4 acc[3][2];
#pragma unroll
    for (int m = 0; m < 3; ++m)
#pragma unroll
      for (int c = 0; c < 2; ++c) acc[m][c] = (f32x4){0, 0, 0, 0};
    {
      const char* mb = smem + aM;
      bf16x8 mf[16];
#pragma unroll
      for (int t = 0; t < 3; ++t) mf[t] = *(const bf16x8*)(mb + t * 64);
#pragma unroll
      for (int t = 0; t < 16; ++t) {
        if (t + 3 < 16) mf[t + 3] = *(const bf16x8*)(mb + (t + 3) * 64);
#pragma unroll
        for (int m = 0; m < 3; ++m)
#pragma unroll
          for (int c = 0; c < 2; ++c) acc[m][c] = __builtin_amdgcn_mfma_f32_16x16x32_bf16(wf[m][c][t], mf[t], acc[m][c], 0, 0, 0);
        __builtin_amdgcn_sched_barrier(0);
      }
    }
    FS(2)
    if (c16 < 8) {
#pragma unroll
      for (int c = 0; c < 2; ++c) {
        const unsigned off = aE[c];
        u32x2 vq, vk, vv;
        vq[0] = pack2(acc[0][c][0], acc[0][c][1]); vq[1] = pack2(acc[0][c][2], acc[0][c][3]);
        vk[0] = pack2(acc[1][c][0], acc[1][c][1]); vk[1] = pack2(acc[1][c][2], acc[1][c][3]);
        vv[0] = pack2(acc[2][c][0], acc[2][c][1]); vv[1] = pack2(acc[2][c][2], acc[2][c][3]);
        *(u32x2*)(smem + L_Q + off) = vq;
        *(u32x2*)(smem + L_K + off) = vk;
        *(u32x2*)(smem + L_V + off) = vv;
      }
    }
    FS(3)
    __syncthreads();
    FS(4)
    STAMP(2)
    f32x4 sc[3];
    s16x4 va[2][3];
    {
      bf16x8 qf[4], kf[3][4];
#pragma unroll
      for (int t = 0; t < 4; ++t) {
        qf[t] = *(const bf16x8*)(smem + aQ[t]);
        kf[0][t] = *(const bf16x8*)(smem + aK[t]);
      }
#pragma unroll
      for (int t = 0; t < 4; ++t) kf[1][t] = *(const bf16x8*)(smem + aK[t] + 4096);
      __builtin_amdgcn_sched_barrier(0);
      sc[0] = (f32x4){0, 0, 0, 0};
#pragma unroll
      for (int t = 0; t < 4; ++t) sc[0] = __builtin_amdgcn_mfma_f32_16x16x32_bf16(kf[0][t], qf[t], sc[0], 0, 0, 0);
      __builtin_amdgcn_sched_barrier(0);
#pragma unroll
      for (int t = 0; t < 4; ++t) kf[2][t] = *(const bf16x8*)(smem + aK[t] + 8192);
      sc[1] = (f32x4){0, 0, 0, 0};
#pragma unroll
      for (int t = 0; t < 4; ++t) sc[1] = __builtin_amdgcn_mfma_f32_16x16x32_bf16(kf[1][t], qf[t], sc[1], 0, 0, 0);
      __builtin_amdgcn_sched_barrier(0);
#pragma unroll
      for (int c = 0; c < 2; ++c) {
#pragma unroll
        for (int kt = 0; kt < 3; ++kt)
          va[c][kt] = __builtin_amdgcn_ds_read_tr16_b64_v4i16((LAS s16x4*)(smem + aV[c] + 4096 * kt));
      }
      sc[2] = (f32x4){0, 0, 0, 0};
#pragma unroll
      for (int t = 0; t < 4; ++t) sc[2] = __builtin_amdgcn_mfma_f32_16x16x32_bf16(kf[2][t], qf[t], sc[2], 0, 0, 0);
    }
    __builtin_amdgcn_sched_barrier(0);
    FS(5)
    bf16x8 pf0, pf1;
    {
      const bool hi = c16 >= 8;
      float xv[6];
#pragma unroll
      for (int r = 0; r < 4; ++r) xv[r] = hi ? sc[1][r] : sc[0][r];
      xv[4] = hi ? sc[2][2] : sc[2][0];
      xv[5] = hi ? sc[2][3] : sc[2][1];
      float mx = -3.0e38f;
#pragma unroll
      for (int r = 0; r < 6; ++r) {
        float v = rbf(rbf(xv[r]) * scale);
        if (r >= 4 && g >= 2) v = -3.0e38f;
        xv[r] = v;
        mx = fmaxf(mx, v);
      }
      mx = fmaxf(mx, __uint_as_float(__builtin_amdgcn_update_dpp(0, __float_as_uint(mx), 0x128, 0xf, 0xf, false)));
      {
        u32x2 r = __builtin_amdgcn_permlane16_swap(__float_as_uint(mx), __float_as_uint(mx), false, false);
        mx = fmaxf(__uint_as_float(r[0]), __uint_as_float(r[1]));
        r = __builtin_amdgcn_permlane32_swap(__float_as_uint(mx), __float_as_uint(mx), false, false);
        mx = fmaxf(__uint_as_float(r[0]), __uint_as_float(r[1]));
      }
      const float mxl = mx * 1.4426950408889634f;
      float sum = 0.f;
#pragma unroll
      for (int r = 0; r < 6; ++r) {
        float e = __builtin_amdgcn_exp2f(__builtin_fmaf(xv[r], 1.4426950408889634f, -mxl));
        if (r >= 4 && g >= 2) e = 0.f;
        xv[r] = e;
        sum += e;
      }
      sum += __uint_as_float(__builtin_amdgcn_update_dpp(0, __float_as_uint(sum), 0x128, 0xf, 0xf, false));
      {
        u32x2 r = __builtin_amdgcn_permlane16_swap(__float_as_uint(sum), __float_as_uint(sum), false, false);
        sum = __uint_as_float(r[0]) + __uint_as_float(r[1]);
        r = __builtin_amdgcn_permlane32_swap(__float_as_uint(sum), __float_as_uint(sum), false, false);
        sum = __uint_as_float(r[0]) + __uint_as_float(r[1]);
      }
      const float inv = __builtin_amdgcn_rcpf(sum);
      unsigned k0 = pack2(xv[0] * inv, xv[1] * inv), k1 = pack2(xv[2] * inv, xv[3] * inv), k2 = pack2(xv[4] * inv, xv[5] * inv);
      unsigned o0 = __builtin_amdgcn_update_dpp(0, k0, 0x128, 0xf, 0xf, false);
      unsigned o1 = __builtin_amdgcn_update_dpp(0, k1, 0x128, 0xf, 0xf, false);
      unsigned o2 = __builtin_amdgcn_update_dpp(0, k2, 0x128, 0xf, 0xf, false);
      u32x4 pb0, pb1;
      pb0[0] = k0; pb0[1] = k1; pb0[2] = o0; pb0[3] = o1;
      pb1[0] = k2; pb1[1] = o2; pb1[2] = 0; pb1[3] = 0;
      pf0 = __builtin_bit_cast(bf16x8, pb0); pf1 = __builtin_bit_cast(bf16x8, pb1);
    }
    FS(6)
    {
      f32x4 o[2];
#pragma unroll
      for (int c = 0; c < 2; ++c) {
        s16x8 v0 = __builtin_shufflevector(va[c][0], va[c][1], 0, 1, 2, 3, 4, 5, 6, 7);
        o[c] = __builtin_amdgcn_mfma_f32_16x16x32_bf16(__builtin_bit_cast(bf16x8, v0), pf0, (f32x4){0, 0, 0, 0}, 0, 0, 0);
      }
#pragma unroll
      for (int c = 0; c < 2; ++c) {
        s16x8 v1 = __builtin_shufflevector(va[c][2], va[c][2], 0, 1, 2, 3, 4, 5, 6, 7);
        o[c] = __builtin_amdgcn_mfma_f32_16x16x32_bf16(__builtin_bit_cast(bf16x8, v1), pf1, o[c], 0, 0, 0);
      }
      if (c16 < 8) {
#pragma unroll
        for (int c = 0; c < 2; ++c) {
          u32x2 v; v[0] = pack2(o[c][0], o[c][1]); v[1] = pack2(o[c][2], o[c][3]);
          *(u32x2*)(smem + L_MEM + aE[c]) = v;
        }
      }
    }
    FS(7)
    __syncthreads();
    FS(8)
    STAMP(3)
    if (s + 1 < NSEG) {
      bf16x8 of[4];
#pragma unroll
      for (int t = 0; t < 4; ++t) of[t] = *(const bf16x8*)(smem + aQ[t] + (L_MEM - L_Q));
      const char* wb = smem + aW;
      bf16x8 bw[32];
      constexpr int PD = 8;
      const unsigned base = O_P1 + (unsigned)((((s + 1) * 32 + 8 * w + (c16 >> 3)) * 32 + h) * 128 + (c16 & 7) * 16 + 4 * g) * 4;
#pragma unroll
      for (int u = 0; u < PD; ++u) bw[u] = *(const bf16x8*)(wb + u * 1024);
      f32x4 de, dod;
#pragma unroll
      for (int jp = 0; jp <= 4; ++jp) {
        f32x4 d0, d1;
        if (jp < 4) {
#pragma unroll
          for (int t = 0; t < 8; ++t) {
            const int u = jp * 8 + t;
            if (u + PD < 32) bw[u + PD] = *(const bf16x8*)(wb + (u + PD) * 1024);
            if (t == 0) d0 = __builtin_amdgcn_mfma_f32_16x16x32_bf16(bw[u], of[0], (f32x4){0, 0, 0, 0}, 0, 0, 0);
            else if (t < 4) d0 = __builtin_amdgcn_mfma_f32_16x16x32_bf16(bw[u], of[t], d0, 0, 0, 0);
            else if (t == 4) d1 = __builtin_amdgcn_mfma_f32_16x16x32_bf16(bw[u], of[0], (f32x4){0, 0, 0, 0}, 0, 0, 0);
            else d1 = __builtin_amdgcn_mfma_f32_16x16x32_bf16(bw[u], of[t - 4], d1, 0, 0, 0);
          }
        }
        if (jp > 0) {
          u32x4 v;
#pragma unroll
          for (int r = 0; r < 4; ++r)
            v[r] = 1u | (unsigned)__builtin_amdgcn_update_dpp(__float_as_uint(de[r]), __float_as_uint(dod[r]), 0x128, 0xf, 0xc, false);
          __builtin_amdgcn_raw_buffer_store_b128(v, wr, base + (jp - 1) * (2 * 32 * 128 * 4), 0, 16);
        }
        de = d0; dod = d1;
        __builtin_amdgcn_sched_barrier(0);
      }
      FS(9)
      FS(10)
      STAMP(4)
      {
        int r = tid >> 5, c = tid & 31;
        u32x4 v = *(const u32x4*)(smem + (c < 16 ? L_K : L_V) + swz(r, c & 15));
        __builtin_amdgcn_raw_buffer_store_b128(v, wr, O_KVM + (unsigned)((s * MM + r) * 8192 + (c >> 4) * 4096 + h * 128 + (c & 15) * 8) * 2, 0, 16);
      }
      f32x4 t4;
      while (true) {
        u32x4 a[4]; unsigned ok = 1u;
        asm volatile("" ::: "memory");
#pragma unroll
        for (int i = 0; i < 4; ++i)
          a[i] = __builtin_amdgcn_raw_buffer_load_b128(wr, O_P1 + (unsigned)((((s + 1) * 32 + h) * 32 + i * 8 + (l >> 3)) * 128 + 32 * w + (l & 7) * 4) * 4, 0, 16);
        t4 = (f32x4){0, 0, 0, 0};
#pragma unroll
        for (int i = 0; i < 4; ++i) {
          ok &= a[i][0] & a[i][1] & a[i][2] & a[i][3];
#pragma unroll
          for (int r = 0; r < 4; ++r) t4[r] += __uint_as_float(a[i][r]);
        }
        if (__all(ok & 1u) || ++spins > (1u << 20)) break;
      }
      FS(11)
      STAMP(5)
      u32x4 v;
#pragma unroll
      for (int r = 0; r < 4; ++r) {
        float x = t4[r];
        x += __uint_as_float(__builtin_amdgcn_update_dpp(0, __float_as_uint(x), 0x128  , 0xf, 0xf, false));
        u32x2 q = __builtin_amdgcn_permlane16_swap(__float_as_uint(x), __float_as_uint(x), false, false);
        x = __uint_as_float(q[0]) + __uint_as_float(q[1]);
        q = __builtin_amdgcn_permlane32_swap(__float_as_uint(x), __float_as_uint(x), false, false);
        x = __uint_as_float(q[0]) + __uint_as_float(q[1]);
        v[r] = __float_as_uint(x) | 1u;
      }
      if (l < 8) __builtin_amdgcn_raw_buffer_store_b128(v, wr, O_P2 + (unsigned)((s + 1) * 4096 + h * 128 + 32 * w + l * 4) * 4, 0, 16);
      {
        const int wu = __builtin_amdgcn_readfirstlane(w);
#pragma unroll
        for (int i = 0; i < 2; ++i) {
          const int r4 = wu * 8 + i * 4;
          const int rr = r4 + (l >> 4), row = 8 + rr;
          const int cc = (l & 15) ^ (((row & 3) << 2) | ((row >> 2) & 3));
          const char* src = (const char*)p.qkv + ((size_t)((s + 1) * SEG + rr) * NQKV + 4096 + h * 128 + cc * 8) * 2;
          __builtin_amdgcn_global_load_lds((const void*)src, (LAS void*)(smem + L_K + (8 + r4) * 256), 16, 0, 16);
          __builtin_amdgcn_global_load_lds((const void*)(src + 8192), (LAS void*)(smem + L_V + (8 + r4) * 256), 16, 0, 16);
        }
      }
      FS(12)
      STAMP(6)
    } else {
      int r = tid >> 5, c = tid & 31;
      u32x4 v = *(const u32x4*)(smem + (c < 16 ? L_K : L_V) + swz(r, c & 15));
      __builtin_amdgcn_raw_buffer_store_b128(v, wr, O_KVM + (unsigned)((s * MM + r) * 8192 + (c >> 4) * 4096 + h * 128 + (c & 15) * 8) * 2, 0, 16);
      asm volatile("s_waitcnt vmcnt(0)" ::: "memory");
      __syncthreads();
      if (threadIdx.x == 0) __hip_atomic_fetch_add(fl + F_M + s * 16, 1u, RLX_AGENT);
    }
  }
}

__device__ __forceinline__ void task_attn(const Params& p, char* smem, int s, int hg) {
  const int tid = threadIdx.x, w = tid >> 6, l = tid & 63, g = l >> 4, c16 = l & 15;
  const int q4 = c16 >> 2, p4 = c16 & 3;
  typedef __attribute__((ext_vector_type(8))) short s16x8;
  const u16* __restrict__ qkv = p.qkv;
  const u16* __restrict__ kvm = p.kvm;
  const float scale = 0.08838834764831845f;
  char* sv = smem + w * 48 * 256;
  const int h = hg * 4 + w;
  u32x4 vst[12];
#pragma unroll
  for (int i = 0; i < 12; ++i) {
    int ch = l + i * 64, r = ch >> 4, c = ch & 15;
    u32x4 v = (u32x4){0, 0, 0, 0};
    if (r < 8) v = *(const u32x4*)(kvm + (size_t)(s * MM + r) * 8192 + 4096 + h * 128 + c * 8);
    else if (r < 40) v = *(const u32x4*)(qkv + (size_t)(s * SEG + r - 8) * NQKV + 8192 + h * 128 + c * 8);
    vst[i] = v;
  }
  bf16x8 qf[4][2], kf[4][3];
#pragma unroll
  for (int t = 0; t < 4; ++t) {
#pragma unroll
    for (int qt = 0; qt < 2; ++qt)
      qf[t][qt] = *(const bf16x8*)(qkv + (size_t)(s * SEG + 16 * qt + c16) * NQKV + h * 128 + 32 * t + 8 * g);
#pragma unroll
    for (int kt = 0; kt < 3; ++kt) {
      int key = 16 * kt + c16;
      const u16* src = key < 8 ? kvm + (size_t)(s * MM + key) * 8192 + h * 128
                               : qkv + (size_t)(s * SEG + (key < 40 ? key - 8 : 0)) * NQKV + 4096 + h * 128;
      kf[t][kt] = *(const bf16x8*)(src + 32 * t + 8 * g);
    }
  }
#pragma unroll
  for (int i = 0; i < 12; ++i) {
    int ch = l + i * 64, r = ch >> 4, c = ch & 15;
    *(u32x4*)(sv + swz(r, c)) = vst[i];
  }
  f32x4 sc[3][2];
#pragma unroll
  for (int kt = 0; kt < 3; ++kt)
#pragma unroll
    for (int qt = 0; qt < 2; ++qt) sc[kt][qt] = (f32x4){0, 0, 0, 0};
#pragma unroll
  for (int t = 0; t < 4; ++t)
#pragma unroll
    for (int kt = 0; kt < 3; ++kt)
#pragma unroll
      for (int qt = 0; qt < 2; ++qt) sc[kt][qt] = __builtin_amdgcn_mfma_f32_16x16x32_bf16(kf[t][kt], qf[t][qt], sc[kt][qt], 0, 0, 0);
  bf16x8 pf0[2], pf1[2];
#pragma unroll
  for (int qt = 0; qt < 2; ++qt) {
    int qi = 8 + 16 * qt + c16;
    float mx = -3.0e38f;
#pragma unroll
    for (int kt = 0; kt < 3; ++kt)
#pragma unroll
      for (int r = 0; r < 4; ++r) {
        int key = 16 * kt + 4 * g + r;
        float v = rbf(rbf(sc[kt][qt][r]) * scale);
        if (key > qi) v = -1e9f;
        if (key >= 40) v = -3.0e38f;
        sc[kt][qt][r] = v;
        mx = fmaxf(mx, v);
      }
    mx = fmaxf(mx, __shfl_xor(mx, 16));
    mx = fmaxf(mx, __shfl_xor(mx, 32));
    float sum = 0.f;
#pragma unroll
    for (int kt = 0; kt < 3; ++kt)
#pragma unroll
      for (int r = 0; r < 4; ++r) {
        int key = 16 * kt + 4 * g + r;
        float e = (key >= 40) ? 0.f : __expf(sc[kt][qt][r] - mx);
        sc[kt][qt][r] = e;
        sum += e;
      }
    sum += __shfl_xor(sum, 16);
    sum += __shfl_xor(sum, 32);
    float inv = 1.f / sum;
    u32x4 b0, b1;
    b0[0] = pack2(sc[0][qt][0] * inv, sc[0][qt][1] * inv); b0[1] = pack2(sc[0][qt][2] * inv, sc[0][qt][3] * inv);
    b0[2] = pack2(sc[1][qt][0] * inv, sc[1][qt][1] * inv); b0[3] = pack2(sc[1][qt][2] * inv, sc[1][qt][3] * inv);
    b1[0] = pack2(sc[2][qt][0] * inv, sc[2][qt][1] * inv); b1[1] = pack2(sc[2][qt][2] * inv, sc[2][qt][3] * inv);
    b1[2] = 0; b1[3] = 0;
    pf0[qt] = __builtin_bit_cast(bf16x8, b0);
    pf1[qt] = __builtin_bit_cast(bf16x8, b1);
  }
  __builtin_amdgcn_s_waitcnt(0xc07f);
  __builtin_amdgcn_wave_barrier();
#pragma unroll
  for (int c = 0; c < 8; ++c) {
    int ch = 2 * c + (p4 >> 1);
    s16x4 a0 = __builtin_amdgcn_ds_read_tr16_b64_v4i16((LAS s16x4*)(sv + swz(4 * g + q4, ch) + 8 * (p4 & 1)));
    s16x4 a1 = __builtin_amdgcn_ds_read_tr16_b64_v4i16((LAS s16x4*)(sv + swz(16 + 4 * g + q4, ch) + 8 * (p4 & 1)));
    s16x4 a2 = __builtin_amdgcn_ds_read_tr16_b64_v4i16((LAS s16x4*)(sv + swz(32 + 4 * g + q4, ch) + 8 * (p4 & 1)));
    s16x8 v0 = __builtin_shufflevector(a0, a1, 0, 1, 2, 3, 4, 5, 6, 7);
    s16x8 v1 = __builtin_shufflevector(a2, a2, 0, 1, 2, 3, 4, 5, 6, 7);
#pragma unroll
    for (int qt = 0; qt < 2; ++qt) {
      f32x4 o = (f32x4){0, 0, 0, 0};
      o = __builtin_amdgcn_mfma_f32_16x16x32_bf16(__builtin_bit_cast(bf16x8, v0), pf0[qt], o, 0, 0, 0);
      o = __builtin_amdgcn_mfma_f32_16x16x32_bf16(__builtin_bit_cast(bf16x8, v1), pf1[qt], o, 0, 0, 0);
      u32x2 v; v[0] = pack2(o[0], o[1]); v[1] = pack2(o[2], o[3]);
      st8_sc1(p.ox + (size_t)(s * SEG + 16 * qt + c16) * 4096 + h * 128 + 16 * c + 4 * g, v);
    }
  }
}

static constexpr int E_WO = 0;
static constexpr int E_RED = 131072;
__device__ __forceinline__ void load_wo_slice(const Params& p, char* smem, int cb) {
  const int tid = threadIdx.x, w = tid >> 6, l = tid & 63, g = l >> 4, c16 = l & 15;
  const int q4 = c16 >> 2, p4 = c16 & 3;
  typedef __attribute__((ext_vector_type(8))) short s16x8;
  char* st = smem + E_RED;
  for (int r8 = 0; r8 < 8; ++r8) {
    u32x4 v[4];
#pragma unroll
    for (int i = 0; i < 4; ++i) {
      int ch = tid + i * 256, r = ch >> 1, c = ch & 1;
      v[i] = *(const u32x4*)(p.Wo + (size_t)(r8 * 512 + r) * DM + cb * 16 + c * 8);
    }
#pragma unroll
    for (int i = 0; i < 4; ++i) {
      int ch = tid + i * 256, r = ch >> 1, c = ch & 1;
      *(u32x4*)(st + r * 32 + c * 16) = v[i];
    }
    __syncthreads();
#pragma unroll
    for (int t = 0; t < 4; ++t) {
      int ks = 4 * w + t;
      int r0 = 32 * ks + 8 * g + q4;
      s16x4 lo = __builtin_amdgcn_ds_read_tr16_b64_v4i16((LAS s16x4*)(st + r0 * 32 + p4 * 8));
      s16x4 hi = __builtin_amdgcn_ds_read_tr16_b64_v4i16((LAS s16x4*)(st + (r0 + 4) * 32 + p4 * 8));
      s16x8 tt = __builtin_shufflevector(lo, hi, 0, 1, 2, 3, 4, 5, 6, 7);
      *(s16x8*)(smem + E_WO + (r8 * 16 + ks) * 1024 + l * 16) = tt;
    }
    __syncthreads();
  }
}
__device__ __forceinline__ void task_out(const Params& p, char* smem, int s, int cb) {
  const int tid = threadIdx.x, w = tid >> 6, l = tid & 63, g = l >> 4, c16 = l & 15;
  f32x4 acc[2];
  acc[0] = (f32x4){0, 0, 0, 0}; acc[1] = (f32x4){0, 0, 0, 0};
  const u16* a0 = p.ox + (size_t)(s * SEG + c16) * 4096 + w * 1024 + 8 * g;
  const u16* a1 = a0 + 16 * 4096;
  const char* wb = smem + E_WO + w * 32768 + l * 16;
#pragma unroll
  for (int hb = 0; hb < 2; ++hb) {
    bf16x8 x0[16], x1[16];
#pragma unroll
    for (int t = 0; t < 16; ++t) { x0[t] = *(const bf16x8*)(a0 + 32 * (hb * 16 + t)); x1[t] = *(const bf16x8*)(a1 + 32 * (hb * 16 + t)); }
    __builtin_amdgcn_sched_barrier(0);
#pragma unroll
    for (int t = 0; t < 16; ++t) {
      bf16x8 wf = *(const bf16x8*)(wb + (hb * 16 + t) * 1024);
      acc[0] = __builtin_amdgcn_mfma_f32_16x16x32_bf16(wf, x0[t], acc[0], 0, 0, 0);
      acc[1] = __builtin_amdgcn_mfma_f32_16x16x32_bf16(wf, x1[t], acc[1], 0, 0, 0);
    }
    __builtin_amdgcn_sched_barrier(0);
  }
  *(f32x4*)(smem + E_RED + (w * 2 + 0) * 1024 + l * 16) = acc[0];
  *(f32x4*)(smem + E_RED + (w * 2 + 1) * 1024 + l * 16) = acc[1];
  __syncthreads();
  if (w < 2) {
    f32x4 a = *(f32x4*)(smem + E_RED + w * 1024 + l * 16);
#pragma unroll
    for (int i = 1; i < 4; ++i) a += *(f32x4*)(smem + E_RED + (i * 2 + w) * 1024 + l * 16);
    int m = s * SEG + 16 * w + c16, n = cb * 16 + 4 * g;
    u32x2 gg = *(const u32x2*)(p.g + (size_t)m * DM + n), xx = *(const u32x2*)(p.x + (size_t)m * DM + n);
    float gf[4] = {bf2f(gg[0] & 0xffff), bf2f(gg[0] >> 16), bf2f(gg[1] & 0xffff), bf2f(gg[1] >> 16)};
    float xf[4] = {bf2f(xx[0] & 0xffff), bf2f(xx[0] >> 16), bf2f(xx[1] & 0xffff), bf2f(xx[1] >> 16)};
    float r[4];
#pragma unroll
    for (int i = 0; i < 4; ++i) r[i] = xf[i] + rbf(rbf(a[i]) * gf[i]);
    u32x2 o; o[0] = pack2(r[0], r[1]); o[1] = pack2(r[2], r[3]);
    *(u32x2*)(p.out + (size_t)m * DM + n) = o;
  }
  __syncthreads();
}

__device__ __forceinline__ void worker(const Params& p, char* smem) {
  constexpr int NWK = 224;
  const int wid = blockIdx.x - NH;
  {
    const __amdgpu_buffer_rsrc_t wr = __builtin_amdgcn_make_buffer_rsrc(p.fl, 0, O_END, 0x00020000);
    constexpr unsigned NCH = NSEG * 32 * 32 * 128 * 4 / 16;
    for (unsigned i = wid * 256 + threadIdx.x; i < NCH; i += NWK * 256)
      __builtin_amdgcn_raw_buffer_store_b128((u32x4){0, 0, 0, 0}, wr, O_P1 + i * 16, 0, 16);
    publish(p.fl + F_Z, nullptr);
  }
  for (int t = wid; t < 1024; t += NWK) {
    int rb = t >> 6, ct = t & 63;
    const u16* B = ct < 32 ? p.Wk : p.Wv;
    EpiStore epi{p.qkv + 4096 + (ct >> 5) * 4096, NQKV, 0};
    gemm_tile<128, 128, 64, EpiStore>(smem, p.xn, DM, B, 4096, S_, 4096, DM, rb * 128, (ct & 31) * 128, epi);
    publish(p.fl + F_TILE + t, p.fl + F_QKV + rb * 16);
  }
  for (int t = (wid + 224 - (1024 % NWK)) % NWK; t < 176; t += NWK) {
    int rb = t / 11, ct = t % 11;
    EpiGelu epi{p.h1, FF, 0, p.b1};
    gemm_tile<128, 128, 64, EpiGelu>(smem, p.y, DM, p.w1, FF, S_, FF, DM, rb * 128, ct * 128, epi);
    publish(p.fl + F_H + rb * 16, nullptr);
  }
  for (int t = (wid + 48) % NWK; t < 512; t += NWK) {
    int rb = t >> 5, ct = t & 31;
    EpiStore epi{p.qkv, NQKV, 0};
    gemm_tile<128, 128, 64, EpiStore>(smem, p.xn, DM, p.Wq, 4096, S_, 4096, DM, rb * 128, ct * 128, epi);
    publish(p.fl + F_QKV + rb * 16, nullptr);
  }
  for (int t = (wid + 120) % NWK; t < 64; t += NWK) {
    int rb = t >> 2, ct = t & 3;
    if (threadIdx.x == 0) wait1<64>(p.fl + F_H + rb * 16, 11u);
    acquire_all();
    EpiSilu epi{p.g, DM, 0, p.b2};
    gemm_tile<128, 128, 64, EpiSilu>(smem, p.h1, FF, p.w2, DM, S_, DM, FF, rb * 128, ct * 128, epi);
    publish(p.fl + F_G + rb * 16, nullptr);
  }
  if (wid < 32) {
    for (int s = wid >> 3; s < NSEG; s += 4) {
      if (threadIdx.x == 0) {
        wait1<64>(p.fl + F_QKV + (s >> 2) * 16, 96u);
        if (s >= 60) wait1<8>(p.fl + F_M + s * 16, 32u); else wait1<64>(p.fl + F_M + s * 16, 32u);
      }
      acquire_all();
      task_attn(p, smem, s, wid & 7);
      asm volatile("s_waitcnt vmcnt(0)" ::: "memory");
      __syncthreads();
      if (threadIdx.x == 0) {
        unsigned old = __hip_atomic_fetch_add(p.fl + F_OX + s * 16, 1u, RLX_AGENT);
        if (old == 7u)
          for (int c = 0; c < 32; ++c) __hip_atomic_store(p.fl + F_OXB + (s * 32 + c) * 16, 1u, RLX_AGENT);
      }
    }
  } else {
    const int j = wid - 32, cb = j & 31, grp = j >> 5;
    load_wo_slice(p, smem, cb);
    for (int s = grp; s < NSEG; s += 6) {
      if (threadIdx.x == 0) {
        wait1<64>(p.fl + F_G + (s >> 2) * 16, 4u);
        if (s >= 60) wait1<8>(p.fl + F_OXB + (s * 32 + cb) * 16, 1u); else wait1<64>(p.fl + F_OXB + (s * 32 + cb) * 16, 1u);
      }
      acquire_all();
      task_out(p, smem, s, cb);
    }
  }
}

__global__ __launch_bounds__(256, 1) void k_main(Params p) {
  extern __shared__ __attribute__((aligned(16))) char smem[];
  if (blockIdx.x < NH) recurrence(p, smem);
  else worker(p, smem);
}

extern "C" void kernel_launch(void* const* d_in, const int* in_sizes, int n_in, void* d_out, int out_size, void* d_ws,
                              size_t ws_size, hipStream_t stream) {
  Params p;
  p.x = (const u16*)d_in[0];
  p.mem0 = (const u16*)d_in[1];
  p.Wq = (const u16*)d_in[2];
  p.Wk = (const u16*)d_in[3];
  p.Wv = (const u16*)d_in[4];
  p.Wo = (const u16*)d_in[5];
  p.w1 = (const u16*)d_in[8];
  p.b1 = (const u16*)d_in[9];
  p.w2 = (const u16*)d_in[10];
  p.b2 = (const u16*)d_in[11];
  p.out = (u16*)d_out;
  char* ws = (char*)d_ws;
  size_t o = 0;
  auto take = [&](size_t bytes) { char* q = ws + o; o += (bytes + 255) & ~(size_t)255; return q; };
  p.fl = (unsigned*)take(F_WORDS * 4);
  p.P2 = (float*)take((size_t)NSEG * 4096 * 4);
  p.P1 = (float*)take((size_t)NSEG * 32 * 32 * 128 * 4);
  const size_t zbytes = o;
  p.xn = (u16*)take((size_t)S_ * DM * 2);
  p.y = (u16*)take((size_t)S_ * DM * 2);
  p.qkv = (u16*)take((size_t)S_ * NQKV * 2);
  p.h1 = (u16*)take((size_t)S_ * FF * 2);
  p.g = (u16*)take((size_t)S_ * DM * 2);
  p.kvm = (u16*)take((size_t)NSEG * MM * 8192 * 2);
  p.ox = (u16*)take((size_t)S_ * 4096 * 2);
  p.stamps = (unsigned long long*)(ws + 200 * 1024 * 1024);

  k_prep<<<512, 256, 0, stream>>>(p.x, p.xn, p.y, (float*)ws, (int)(O_P1 / 16));
  hipFuncSetAttribute((const void*)k_main, hipFuncAttributeMaxDynamicSharedMemorySize, 163840);
  k_main<<<256, 256, 163840, stream>>>(p);
}
```

```cpp
#include <hip/hip_runtime.h>
#include <hip/hip_bf16.h>
#include <stdint.h>

typedef __attribute__((ext_vector_type(8))) __bf16 bf16x8;
typedef __attribute__((ext_vector_type(4))) __bf16 bf16x4;
typedef __attribute__((ext_vector_type(4))) float f32x4;
typedef __attribute__((ext_vector_type(4))) short s16x4;
typedef __attribute__((ext_vector_type(4))) unsigned u32x4;
typedef __attribute__((ext_vector_type(2))) unsigned u32x2;
typedef unsigned short u16;
#define LAS __attribute__((address_space(3)))

static constexpr int S_ = 2048, DM = 512, NH = 32, DH = 128, MM = 8, SEG = 32, NSEG = 64, FF = 1376;
static constexpr int NQKV = 3 * NH * DH;

__device__ __forceinline__ float bf2f(u16 v) { return __uint_as_float(((unsigned)v) << 16); }
__device__ __forceinline__ u16 f2bf(float f) {
  __bf16 b = (__bf16)f;
  return __builtin_bit_cast(u16, b);
}
__device__ __forceinline__ float rbf(float f) { return bf2f(f2bf(f)); }
typedef __attribute__((ext_vector_type(2))) float f32x2;
typedef __attribute__((ext_vector_type(2))) __bf16 bf16x2;
__device__ __forceinline__ unsigned pack2(float a, float b) {
  bf16x2 r = __builtin_convertvector((f32x2){a, b}, bf16x2);
  return __builtin_bit_cast(unsigned, r);
}

__global__ __launch_bounds__(256) void k_prep(const u16* __restrict__ x, u16* __restrict__ xn, u16* __restrict__ y,
                                              float* __restrict__ zb, int zn4) {
  int tid = threadIdx.x, w = tid >> 6, l = tid & 63;
  int row = blockIdx.x * 4 + w;
  {
    int gt = blockIdx.x * 256 + tid;
    for (int i = gt; i < zn4; i += 512 * 256) ((f32x4*)zb)[i] = (f32x4){0, 0, 0, 0};
  }
  u32x4 raw = *(const u32x4*)(x + (size_t)row * DM + l * 8);
  float v[8];
#pragma unroll
  for (int i = 0; i < 4; ++i) { v[2 * i] = bf2f(raw[i] & 0xffff); v[2 * i + 1] = bf2f(raw[i] >> 16); }
#pragma unroll
  for (int pass = 0; pass < 2; ++pass) {
    float s = 0.f;
#pragma unroll
    for (int i = 0; i < 8; ++i) s += v[i];
#pragma unroll
    for (int m = 32; m >= 1; m >>= 1) s += __shfl_xor(s, m);
    float mean = s * (1.f / DM);
    float q = 0.f;
#pragma unroll
    for (int i = 0; i < 8; ++i) { float d = v[i] - mean; q += d * d; }
#pragma unroll
    for (int m = 32; m >= 1; m >>= 1) q += __shfl_xor(q, m);
    float var = rbf(q * (1.f / DM));
    float mb = rbf(mean);
    float den = rbf(sqrtf(rbf(var + 1e-5f)));
#pragma unroll
    for (int i = 0; i < 8; ++i) v[i] = rbf(rbf(v[i] - mb) / den);
    u32x4 o;
#pragma unroll
    for (int i = 0; i < 4; ++i) o[i] = pack2(v[2 * i], v[2 * i + 1]);
    *(u32x4*)((pass == 0 ? xn : y) + (size_t)row * DM + l * 8) = o;
  }
}


struct Params {
  const u16 *x, *mem0, *Wq, *Wk, *Wv, *Wo, *w1, *b1, *w2, *b2;
  u16 *out, *xn, *y, *qkv, *h1, *g, *kvm, *ox;
  float *P1, *P2;
  unsigned* fl;
};
#define RLX_AGENT __ATOMIC_RELAXED, __HIP_MEMORY_SCOPE_AGENT
static constexpr int F_TILE = 0;
static constexpr int F_QKV = 1024;
static constexpr int F_H = 1280;
static constexpr int F_G = 1536;
static constexpr int F_M = 2048;
static constexpr int F_OX = 3072;
static constexpr int F_OXB = 4096;
static constexpr int F_WORDS = 4096 + 64 * 32 * 16;
static constexpr unsigned O_FL = 0;
static constexpr unsigned O_P2 = F_WORDS * 4;
static constexpr unsigned O_P1 = O_P2 + NSEG * 4096 * 4;
static constexpr unsigned O_ZEND = O_P1 + NSEG * 32 * 32 * 128 * 4;
static constexpr unsigned O_XN = O_ZEND;
static constexpr unsigned O_Y = O_XN + S_ * DM * 2;
static constexpr unsigned O_QKV = O_Y + S_ * DM * 2;
static constexpr unsigned O_H1 = O_QKV + S_ * NQKV * 2;
static constexpr unsigned O_G = O_H1 + S_ * FF * 2;
static constexpr unsigned O_KVM = O_G + S_ * DM * 2;
static constexpr unsigned O_END = O_KVM + NSEG * MM * 8192 * 2;

__device__ __forceinline__ void st8_sc1(u16* p, u32x2 v) {
  __hip_atomic_store((unsigned long long*)p, (unsigned long long)v[0] | ((unsigned long long)v[1] << 32), RLX_AGENT);
}
__device__ __forceinline__ void publish(unsigned* c1, unsigned* c2) {
  asm volatile("s_waitcnt vmcnt(0)" ::: "memory");
  __syncthreads();
  if (threadIdx.x == 0) {
    __hip_atomic_fetch_add(c1, 1u, RLX_AGENT);
    if (c2) __hip_atomic_fetch_add(c2, 1u, RLX_AGENT);
  }
}
template <int SLP = 4>
__device__ __forceinline__ void wait1(unsigned* c, unsigned target) {
  unsigned spins = 0;
  while (__hip_atomic_load(c, RLX_AGENT) < target) {
    __builtin_amdgcn_s_sleep(SLP);
    if (++spins > (1u << 22)) break;
  }
}
__device__ __forceinline__ void acquire_all() {
  if (threadIdx.x == 0) {
    __builtin_amdgcn_fence(__ATOMIC_ACQUIRE, "agent");
    asm volatile("s_waitcnt vmcnt(0)" ::: "memory");
  }
  __syncthreads();
}

struct EpiStore {
  u16* C; int ldc; int pad_;
  __device__ __forceinline__ void operator()(int m, int n, f32x4 a) const {
    u32x2 o; o[0] = pack2(a[0], a[1]); o[1] = pack2(a[2], a[3]);
    st8_sc1(C + (size_t)m * ldc + n, o);
  }
};
struct EpiGelu {
  u16* C; int ldc; int pad_; const u16* b;
  __device__ __forceinline__ void operator()(int m, int n, f32x4 a) const {
    float r[4];
#pragma unroll
    for (int i = 0; i < 4; ++i) {
      float v = rbf(rbf(a[i]) + bf2f(b[n + i]));
      float u = 0.7978845608028654f * (v + 0.044715f * v * v * v);
      r[i] = 0.5f * v * (1.f + tanhf(u));
    }
    u32x2 o; o[0] = pack2(r[0], r[1]); o[1] = pack2(r[2], r[3]);
    st8_sc1(C + (size_t)m * ldc + n, o);
  }
};
struct EpiSilu {
  u16* C; int ldc; int pad_; const u16* b;
  __device__ __forceinline__ void operator()(int m, int n, f32x4 a) const {
    float r[4];
#pragma unroll
    for (int i = 0; i < 4; ++i) {
      float v = rbf(rbf(a[i]) + bf2f(b[n + i]));
      r[i] = v / (1.f + __expf(-v));
    }
    u32x2 o; o[0] = pack2(r[0], r[1]); o[1] = pack2(r[2], r[3]);
    st8_sc1(C + (size_t)m * ldc + n, o);
  }
};

template <int BM, int BN, int BK>
struct GemmCfg {
  static constexpr int ASTR = BK * 2 + 16;
  static constexpr int BSTR = BN * 2 + 32;
  static constexpr int ABYTES = BM * ASTR, BBYTES = BK * BSTR;
  static constexpr int LDS = 2 * (ABYTES + BBYTES);
};

template <int BM, int BN, int BK, class Epi>
__device__ __forceinline__ void gemm_tile(char* smem, const u16* __restrict__ A, int lda, const u16* __restrict__ B, int ldb,
                                          int M, int N, int K, int bm, int bn, const Epi& epi) {
  using C = GemmCfg<BM, BN, BK>;
  constexpr int WM = BM / 2, WN = BN / 2, TM = WM / 16, TN = WN / 16;
  constexpr int ACH = BM * BK / 8 / 256;
  constexpr int BCH = BK * BN / 8 / 256;
  static_assert(ACH >= 1 && BCH >= 1, "tile too small");
  const int tid = threadIdx.x, w = tid >> 6, l = tid & 63, g = l >> 4, c16 = l & 15;
  const int wm = w >> 1, wn = w & 1;
  f32x4 acc[TM][TN];
#pragma unroll
  for (int i = 0; i < TM; ++i)
#pragma unroll
    for (int j = 0; j < TN; ++j) acc[i][j] = (f32x4){0, 0, 0, 0};
  u32x4 ra[ACH], rb[BCH];
  auto gload = [&](int k0) {
#pragma unroll
    for (int i = 0; i < ACH; ++i) {
      int ch = tid + i * 256, r = ch / (BK / 8), c = ch % (BK / 8);
      int k = k0 + c * 8;
      ra[i] = (k < K) ? *(const u32x4*)(A + (size_t)(bm + r) * lda + k) : (u32x4){0, 0, 0, 0};
    }
#pragma unroll
    for (int i = 0; i < BCH; ++i) {
      int ch = tid + i * 256, r = ch / (BN / 8), c = ch % (BN / 8);
      int k = k0 + r, n = bn + c * 8;
      rb[i] = (k < K && n < N) ? *(const u32x4*)(B + (size_t)k * ldb + n) : (u32x4){0, 0, 0, 0};
    }
  };
  auto lstore = [&](int buf) {
    char* sa = smem + buf * (C::ABYTES + C::BBYTES);
    char* sb = sa + C::ABYTES;
#pragma unroll
    for (int i = 0; i < ACH; ++i) {
      int ch = tid + i * 256, r = ch / (BK / 8), c = ch % (BK / 8);
      *(u32x4*)(sa + r * C::ASTR + c * 16) = ra[i];
    }
#pragma unroll
    for (int i = 0; i < BCH; ++i) {
      int ch = tid + i * 256, r = ch / (BN / 8), c = ch % (BN / 8);
      *(u32x4*)(sb + r * C::BSTR + ((c ^ (((r >> 3) & 1) * (BN / 16))) * 16)) = rb[i];
    }
  };
  const int nk = (K + BK - 1) / BK;
  gload(0);
  lstore(0);
  __syncthreads();
  for (int kt = 0; kt < nk; ++kt) {
    int buf = kt & 1;
    if (kt + 1 < nk) gload((kt + 1) * BK);
    const char* sa = smem + buf * (C::ABYTES + C::BBYTES);
    const char* sb = sa + C::ABYTES;
#pragma unroll
    for (int ks = 0; ks < BK / 32; ++ks) {
      bf16x8 af[TM], bfr[TN];
#pragma unroll
      for (int i = 0; i < TM; ++i)
        af[i] = *(const bf16x8*)(sa + (wm * WM + i * 16 + c16) * C::ASTR + (ks * 32 + g * 8) * 2);
#pragma unroll
      for (int j = 0; j < TN; ++j) {
        int q = c16 >> 2, p = c16 & 3;
        int col = wn * WN + j * 16 + p * 4;
        s16x4 lo, hi;
        {
          int r = ks * 32 + g * 8 + q;
          lo = __builtin_amdgcn_ds_read_tr16_b64_v4i16((LAS s16x4*)(sb + r * C::BSTR + ((col * 2) ^ (((r >> 3) & 1) * BN))));
          r += 4;
          hi = __builtin_amdgcn_ds_read_tr16_b64_v4i16((LAS s16x4*)(sb + r * C::BSTR + ((col * 2) ^ (((r >> 3) & 1) * BN))));
        }
        typedef __attribute__((ext_vector_type(8))) short s16x8;
        s16x8 t = __builtin_shufflevector(lo, hi, 0, 1, 2, 3, 4, 5, 6, 7);
        bfr[j] = __builtin_bit_cast(bf16x8, t);
      }
#pragma unroll
      for (int i = 0; i < TM; ++i)
#pragma unroll
        for (int j = 0; j < TN; ++j) acc[i][j] = __builtin_amdgcn_mfma_f32_16x16x32_bf16(bfr[j], af[i], acc[i][j], 0, 0, 0);
    }
    if (kt + 1 < nk) lstore(buf ^ 1);
    __syncthreads();
  }
#pragma unroll
  for (int i = 0; i < TM; ++i)
#pragma unroll
    for (int j = 0; j < TN; ++j) {
      int m = bm + wm * WM + i * 16 + c16, n = bn + wn * WN + j * 16 + g * 4;
      if (n < N) epi(m, n, acc[i][j]);
    }
}

__device__ __forceinline__ unsigned swz(unsigned row, unsigned ch) {
  return 256u * row + 16u * (ch ^ (((row & 3) << 2) | ((row >> 2) & 3)));
}

static constexpr int L_WO = 0;
static constexpr int L_V = 131072;
static constexpr int L_K = L_V + 40 * 256;
static constexpr int L_Q = L_K + 40 * 256;
static constexpr int L_MEM = L_Q + 8 * 256;
static constexpr int L_END = L_MEM + 8704;
static_assert(L_END <= 163840, "lds");


__device__ __forceinline__ void recurrence(const Params& p, char* smem) {
  const u16* __restrict__ Wq = p.Wq; const u16* __restrict__ Wk = p.Wk; const u16* __restrict__ Wv = p.Wv;
  const u16* __restrict__ Wo = p.Wo; const u16* __restrict__ mem0 = p.mem0;
  const int h = blockIdx.x;
  const __amdgpu_buffer_rsrc_t wr = __builtin_amdgcn_make_buffer_rsrc(p.fl, 0, O_END, 0x00020000);
  unsigned* const fl = p.fl;
  typedef __attribute__((ext_vector_type(8))) short s16x8;

  bf16x8 wf[3][2][16];
  {
    const int tid = threadIdx.x, w = tid >> 6, l = tid & 63, g = l >> 4, c16 = l & 15;
    const int q4 = c16 >> 2, p4 = c16 & 3;
#pragma unroll
    for (int m = 0; m < 3; ++m) {
      const u16* W = m == 0 ? Wq : (m == 1 ? Wk : Wv);
      {
        const int wu = __builtin_amdgcn_readfirstlane(w);
#pragma unroll
        for (int i = 0; i < 32; ++i) {
          const int r4 = (i * 4 + wu) * 4;
          const int row = r4 + (l >> 4);
          const int cc = (l & 15) ^ (((row & 3) << 2) | ((row >> 2) & 3));
          __builtin_amdgcn_global_load_lds((const void*)(W + (size_t)row * 4096 + h * 128 + cc * 8), (LAS void*)(smem + r4 * 256), 16, 0, 0);
        }
      }
      __syncthreads();
#pragma unroll
      for (int c = 0; c < 2; ++c)
#pragma unroll
        for (int t = 0; t < 16; ++t) {
          int ct = 2 * w + c;
          int r0 = 32 * t + 8 * g + q4;
          s16x4 lo = __builtin_amdgcn_ds_read_tr16_b64_v4i16((LAS s16x4*)(smem + swz(r0, 2 * ct + (p4 >> 1)) + 8 * (p4 & 1)));
          s16x4 hi = __builtin_amdgcn_ds_read_tr16_b64_v4i16((LAS s16x4*)(smem + swz(r0 + 4, 2 * ct + (p4 >> 1)) + 8 * (p4 & 1)));
          s16x8 tt = __builtin_shufflevector(lo, hi, 0, 1, 2, 3, 4, 5, 6, 7);
          wf[m][c][t] = __builtin_bit_cast(bf16x8, tt);
        }
      __syncthreads();
    }
    u32x4 v[8];
#pragma unroll
    for (int i = 0; i < 8; ++i) {
      int ch = tid + i * 256, r = ch >> 6, c = ch & 63;
      v[i] = *(const u32x4*)(Wo + (size_t)(h * 128 + r) * 512 + c * 8);
    }
    for (int t = 0; t < 4; ++t) {
      char* st = smem + 131072;
#pragma unroll
      for (int i = 0; i < 8; ++i) {
        int ch = tid + i * 256, r = ch >> 6, c = ch & 63;
        *(u32x4*)(st + (c >> 4) * 8192 + swz(r, c & 15)) = v[i];
      }
      if (t + 1 < 4) {
#pragma unroll
        for (int i = 0; i < 8; ++i) {
          int ch = tid + i * 256, r = ch >> 6, c = ch & 63;
          v[i] = *(const u32x4*)(Wo + (size_t)(h * 128 + (t + 1) * 32 + r) * 512 + c * 8);
        }
      }
      __syncthreads();
#pragma unroll
      for (int j = 0; j < 8; ++j) {
        int r0 = 8 * g + q4;
        s16x4 lo = __builtin_amdgcn_ds_read_tr16_b64_v4i16((LAS s16x4*)(st + w * 8192 + swz(r0, 2 * j + (p4 >> 1)) + 8 * (p4 & 1)));
        s16x4 hi = __builtin_amdgcn_ds_read_tr16_b64_v4i16((LAS s16x4*)(st + w * 8192 + swz(r0 + 4, 2 * j + (p4 >> 1)) + 8 * (p4 & 1)));
        s16x8 tt = __builtin_shufflevector(lo, hi, 0, 1, 2, 3, 4, 5, 6, 7);
        *(s16x8*)(smem + L_WO + ((w * 8 + j) * 4 + t) * 1024 + l * 16) = tt;
      }
      __syncthreads();
    }
    if (l == 0) { wait1(fl + F_TILE + h, 1u); wait1(fl + F_TILE + 32 + h, 1u); }
    asm volatile("" ::: "memory");
#pragma unroll
    for (int i = 0; i < 2; ++i) {
      int ch = tid + i * 256, r = ch >> 4, c = ch & 15;
      unsigned off = (unsigned)(r * NQKV + 4096 + h * 128 + c * 8) * 2;
      *(u32x4*)(smem + L_K + swz(8 + r, c)) = __builtin_amdgcn_raw_buffer_load_b128(wr, O_QKV + off, 0, 16);
      *(u32x4*)(smem + L_V + swz(8 + r, c)) = __builtin_amdgcn_raw_buffer_load_b128(wr, O_QKV + off + 8192, 0, 16);
    }
  }

  const float scale = 0.08838834764831845f;
  unsigned aQ[4], aK[4], aV[2], aE[2], aM, aW;
  {
    const int tid = threadIdx.x, w = tid >> 6, l = tid & 63, g = l >> 4, c16 = l & 15, q4 = c16 >> 2, p4 = c16 & 3;
#pragma unroll
    for (int t = 0; t < 4; ++t) { aQ[t] = L_Q + swz(c16 & 7, 4 * t + g); aK[t] = L_K + swz(c16, 4 * t + g); }
#pragma unroll
    for (int c = 0; c < 2; ++c) {
      aV[c] = L_V + swz(4 * g + q4, 2 * (2 * w + c) + (p4 >> 1)) + 8 * (p4 & 1);
      aE[c] = 256u * c16 + 16u * ((4 * w + 2 * c + (g >> 1)) ^ (((c16 & 3) << 2) | ((c16 >> 2) & 3))) + (g & 1) * 8;
    }
    aM = L_MEM + (c16 & 7) * 1088 + g * 16;
    aW = L_WO + w * 32768 + l * 16;
  }

  for (int s = 0; s < NSEG; ++s) {
    int tid = threadIdx.x;
    asm volatile("" : "+v"(tid));
    const int w = tid >> 6, l = tid & 63, g = l >> 4, c16 = l & 15;
    const int q4 = c16 >> 2, p4 = c16 & 3;
    unsigned spins = 0;
    if (s == 0) {
#pragma unroll
      for (int i = 0; i < 2; ++i) {
        int ch = tid + i * 256, r = ch >> 6, c = ch & 63;
        u32x4 v = *(const u32x4*)(mem0 + r * 512 + c * 8);
        *(u32x4*)(smem + L_MEM + r * 1088 + c * 16) = v;
      }
    } else {
      u32x4 a[4];
      while (true) {
        unsigned ok = 1u;
        asm volatile("" ::: "memory");
#pragma unroll
        for (int i = 0; i < 4; ++i) a[i] = __builtin_amdgcn_raw_buffer_load_b128(wr, O_P2 + (unsigned)(s * 4096) * 4 + (i * 256 + tid) * 16, 0, 16);
#pragma unroll
        for (int i = 0; i < 4; ++i) ok &= a[i][0] & a[i][1] & a[i][2] & a[i][3];
        if (((s + 1) & 3) == 0 && s + 1 < NSEG) {
          const unsigned fo = O_FL + (unsigned)(F_TILE + ((s + 1) >> 2) * 64 + h) * 4;
          ok &= __builtin_amdgcn_raw_buffer_load_b32(wr, fo, 0, 16) & __builtin_amdgcn_raw_buffer_load_b32(wr, fo + 128, 0, 16);
        }
        if (__all(ok & 1u) || ++spins > (1u << 20)) break;
      }
#pragma unroll
      for (int i = 0; i < 4; ++i) {
        int ch = i * 256 + tid, r = ch >> 5, qy = (ch >> 2) & 7, qd = ch & 3;
        u32x2 v; v[0] = pack2(__uint_as_float(a[i][0]), __uint_as_float(a[i][1])); v[1] = pack2(__uint_as_float(a[i][2]), __uint_as_float(a[i][3]));
        *(u32x2*)(smem + L_MEM + qy * 1088 + (16 * r + 4 * qd) * 2) = v;
      }
    }
    __syncthreads();
    if (s > 0 && threadIdx.x == 0) __hip_atomic_fetch_add(fl + F_M + (s - 1) * 16, 1u, RLX_AGENT);
    f32x4 acc[3][2];
#pragma unroll
    for (int m = 0; m < 3; ++m)
#pragma unroll
      for (int c = 0; c < 2; ++c) acc[m][c] = (f32x4){0, 0, 0, 0};
    {
      const char* mb = smem + aM;
      bf16x8 mf[16];
#pragma unroll
      for (int t = 0; t < 3; ++t) mf[t] = *(const bf16x8*)(mb + t * 64);
#pragma unroll
      for (int t = 0; t < 16; ++t) {
        if (t + 3 < 16) mf[t + 3] = *(const bf16x8*)(mb + (t + 3) * 64);
#pragma unroll
        for (int m = 0; m < 3; ++m)
#pragma unroll
          for (int c = 0; c < 2; ++c) acc[m][c] = __builtin_amdgcn_mfma_f32_16x16x32_bf16(wf[m][c][t], mf[t], acc[m][c], 0, 0, 0);
        __builtin_amdgcn_sched_barrier(0);
      }
    }
    if (c16 < 8) {
#pragma unroll
      for (int c = 0; c < 2; ++c) {
        const unsigned off = aE[c];
        u32x2 vq, vk, vv;
        vq[0] = pack2(acc[0][c][0], acc[0][c][1]); vq[1] = pack2(acc[0][c][2], acc[0][c][3]);
        vk[0] = pack2(acc[1][c][0], acc[1][c][1]); vk[1] = pack2(acc[1][c][2], acc[1][c][3]);
        vv[0] = pack2(acc[2][c][0], acc[2][c][1]); vv[1] = pack2(acc[2][c][2], acc[2][c][3]);
        *(u32x2*)(smem + L_Q + off) = vq;
        *(u32x2*)(smem + L_K + off) = vk;
        *(u32x2*)(smem + L_V + off) = vv;
      }
    }
    __syncthreads();
    f32x4 sc[3];
    s16x4 va[2][3];
    {
      bf16x8 qf[4], kf[3][4];
#pragma unroll
      for (int t = 0; t < 4; ++t) {
        qf[t] = *(const bf16x8*)(smem + aQ[t]);
        kf[0][t] = *(const bf16x8*)(smem + aK[t]);
      }
#pragma unroll
      for (int t = 0; t < 4; ++t) kf[1][t] = *(const bf16x8*)(smem + aK[t] + 4096);
      __builtin_amdgcn_sched_barrier(0);
      sc[0] = (f32x4){0, 0, 0, 0};
#pragma unroll
      for (int t = 0; t < 4; ++t) sc[0] = __builtin_amdgcn_mfma_f32_16x16x32_bf16(kf[0][t], qf[t], sc[0], 0, 0, 0);
      __builtin_amdgcn_sched_barrier(0);
#pragma unroll
      for (int t = 0; t < 4; ++t) kf[2][t] = *(const bf16x8*)(smem + aK[t] + 8192);
      sc[1] = (f32x4){0, 0, 0, 0};
#pragma unroll
      for (int t = 0; t < 4; ++t) sc[1] = __builtin_amdgcn_mfma_f32_16x16x32_bf16(kf[1][t], qf[t], sc[1], 0, 0, 0);
      __builtin_amdgcn_sched_barrier(0);
#pragma unroll
      for (int c = 0; c < 2; ++c) {
#pragma unroll
        for (int kt = 0; kt < 3; ++kt)
          va[c][kt] = __builtin_amdgcn_ds_read_tr16_b64_v4i16((LAS s16x4*)(smem + aV[c] + 4096 * kt));
      }
      sc[2] = (f32x4){0, 0, 0, 0};
#pragma unroll
      for (int t = 0; t < 4; ++t) sc[2] = __builtin_amdgcn_mfma_f32_16x16x32_bf16(kf[2][t], qf[t], sc[2], 0, 0, 0);
    }
    __builtin_amdgcn_sched_barrier(0);
    bf16x8 pf0, pf1;
    {
      const bool hi = c16 >= 8;
      float xv[6];
#pragma unroll
      for (int r = 0; r < 4; ++r) xv[r] = hi ? sc[1][r] : sc[0][r];
      xv[4] = hi ? sc[2][2] : sc[2][0];
      xv[5] = hi ? sc[2][3] : sc[2][1];
      float mx = -3.0e38f;
#pragma unroll
      for (int r = 0; r < 6; ++r) {
        float v = rbf(rbf(xv[r]) * scale);
        if (r >= 4 && g >= 2) v = -3.0e38f;
        xv[r] = v;
        mx = fmaxf(mx, v);
      }
      mx = fmaxf(mx, __uint_as_float(__builtin_amdgcn_update_dpp(0, __float_as_uint(mx), 0x128, 0xf, 0xf, false)));
      {
        u32x2 r = __builtin_amdgcn_permlane16_swap(__float_as_uint(mx), __float_as_uint(mx), false, false);
        mx = fmaxf(__uint_as_float(r[0]), __uint_as_float(r[1]));
        r = __builtin_amdgcn_permlane32_swap(__float_as_uint(mx), __float_as_uint(mx), false, false);
        mx = fmaxf(__uint_as_float(r[0]), __uint_as_float(r[1]));
      }
      const float mxl = mx * 1.4426950408889634f;
      float sum = 0.f;
#pragma unroll
      for (int r = 0; r < 6; ++r) {
        float e = __builtin_amdgcn_exp2f(__builtin_fmaf(xv[r], 1.4426950408889634f, -mxl));
        if (r >= 4 && g >= 2) e = 0.f;
        xv[r] = e;
        sum += e;
      }
      sum += __uint_as_float(__builtin_amdgcn_update_dpp(0, __float_as_uint(sum), 0x128, 0xf, 0xf, false));
      {
        u32x2 r = __builtin_amdgcn_permlane16_swap(__float_as_uint(sum), __float_as_uint(sum), false, false);
        sum = __uint_as_float(r[0]) + __uint_as_float(r[1]);
        r = __builtin_amdgcn_permlane32_swap(__float_as_uint(sum), __float_as_uint(sum), false, false);
        sum = __uint_as_float(r[0]) + __uint_as_float(r[1]);
      }
      const float inv = __builtin_amdgcn_rcpf(sum);
      unsigned k0 = pack2(xv[0] * inv, xv[1] * inv), k1 = pack2(xv[2] * inv, xv[3] * inv), k2 = pack2(xv[4] * inv, xv[5] * inv);
      unsigned o0 = __builtin_amdgcn_update_dpp(0, k0, 0x128, 0xf, 0xf, false);
      unsigned o1 = __builtin_amdgcn_update_dpp(0, k1, 0x128, 0xf, 0xf, false);
      unsigned o2 = __builtin_amdgcn_update_dpp(0, k2, 0x128, 0xf, 0xf, false);
      u32x4 pb0, pb1;
      pb0[0] = k0; pb0[1] = k1; pb0[2] = o0; pb0[3] = o1;
      pb1[0] = k2; pb1[1] = o2; pb1[2] = 0; pb1[3] = 0;
      pf0 = __builtin_bit_cast(bf16x8, pb0); pf1 = __builtin_bit_cast(bf16x8, pb1);
    }
    {
      f32x4 o[2];
#pragma unroll
      for (int c = 0; c < 2; ++c) {
        s16x8 v0 = __builtin_shufflevector(va[c][0], va[c][1], 0, 1, 2, 3, 4, 5, 6, 7);
        o[c] = __builtin_amdgcn_mfma_f32_16x16x32_bf16(__builtin_bit_cast(bf16x8, v0), pf0, (f32x4){0, 0, 0, 0}, 0, 0, 0);
      }
#pragma unroll
      for (int c = 0; c < 2; ++c) {
        s16x8 v1 = __builtin_shufflevector(va[c][2], va[c][2], 0, 1, 2, 3, 4, 5, 6, 7);
        o[c] = __builtin_amdgcn_mfma_f32_16x16x32_bf16(__builtin_bit_cast(bf16x8, v1), pf1, o[c], 0, 0, 0);
      }
      if (c16 < 8) {
#pragma unroll
        for (int c = 0; c < 2; ++c) {
          u32x2 v; v[0] = pack2(o[c][0], o[c][1]); v[1] = pack2(o[c][2], o[c][3]);
          *(u32x2*)(smem + L_MEM + aE[c]) = v;
        }
      }
    }
    __syncthreads();
    if (s + 1 < NSEG) {
      bf16x8 of[4];
#pragma unroll
      for (int t = 0; t < 4; ++t) of[t] = *(const bf16x8*)(smem + aQ[t] + (L_MEM - L_Q));
      const char* wb = smem + aW;
      bf16x8 bw[32];
      constexpr int PD = 8;
      const unsigned base = O_P1 + (unsigned)((((s + 1) * 32 + 8 * w + (c16 >> 3)) * 32 + h) * 128 + (c16 & 7) * 16 + 4 * g) * 4;
#pragma unroll
      for (int u = 0; u < PD; ++u) bw[u] = *(const bf16x8*)(wb + u * 1024);
      f32x4 de, dod;
#pragma unroll
      for (int jp = 0; jp <= 4; ++jp) {
        f32x4 d0, d1;
        if (jp < 4) {
#pragma unroll
          for (int t = 0; t < 8; ++t) {
            const int u = jp * 8 + t;
            if (u + PD < 32) bw[u + PD] = *(const bf16x8*)(wb + (u + PD) * 1024);
            if (t == 0) d0 = __builtin_amdgcn_mfma_f32_16x16x32_bf16(bw[u], of[0], (f32x4){0, 0, 0, 0}, 0, 0, 0);
            else if (t < 4) d0 = __builtin_amdgcn_mfma_f32_16x16x32_bf16(bw[u], of[t], d0, 0, 0, 0);
            else if (t == 4) d1 = __builtin_amdgcn_mfma_f32_16x16x32_bf16(bw[u], of[0], (f32x4){0, 0, 0, 0}, 0, 0, 0);
            else d1 = __builtin_amdgcn_mfma_f32_16x16x32_bf16(bw[u], of[t - 4], d1, 0, 0, 0);
          }
        }
        if (jp > 0) {
          u32x4 v;
#pragma unroll
          for (int r = 0; r < 4; ++r)
            v[r] = 1u | (unsigned)__builtin_amdgcn_update_dpp(__float_as_uint(de[r]), __float_as_uint(dod[r]), 0x128, 0xf, 0xc, false);
          __builtin_amdgcn_raw_buffer_store_b128(v, wr, base + (jp - 1) * (2 * 32 * 128 * 4), 0, 16);
        }
        de = d0; dod = d1;
        __builtin_amdgcn_sched_barrier(0);
      }
      {
        int r = tid >> 5, c = tid & 31;
        u32x4 v = *(const u32x4*)(smem + (c < 16 ? L_K : L_V) + swz(r, c & 15));
        __builtin_amdgcn_raw_buffer_store_b128(v, wr, O_KVM + (unsigned)((s * MM + r) * 8192 + (c >> 4) * 4096 + h * 128 + (c & 15) * 8) * 2, 0, 16);
      }
      f32x4 t4;
      while (true) {
        u32x4 a[4]; unsigned ok = 1u;
        asm volatile("" ::: "memory");
#pragma unroll
        for (int i = 0; i < 4; ++i)
          a[i] = __builtin_amdgcn_raw_buffer_load_b128(wr, O_P1 + (unsigned)((((s + 1) * 32 + h) * 32 + i * 8 + (l >> 3)) * 128 + 32 * w + (l & 7) * 4) * 4, 0, 16);
        t4 = (f32x4){0, 0, 0, 0};
#pragma unroll
        for (int i = 0; i < 4; ++i) {
          ok &= a[i][0] & a[i][1] & a[i][2] & a[i][3];
#pragma unroll
          for (int r = 0; r < 4; ++r) t4[r] += __uint_as_float(a[i][r]);
        }
        if (__all(ok & 1u) || ++spins > (1u << 20)) break;
      }
      u32x4 v;
#pragma unroll
      for (int r = 0; r < 4; ++r) {
        float x = t4[r];
        x += __uint_as_float(__builtin_amdgcn_update_dpp(0, __float_as_uint(x), 0x128  , 0xf, 0xf, false));
        u32x2 q = __builtin_amdgcn_permlane16_swap(__float_as_uint(x), __float_as_uint(x), false, false);
        x = __uint_as_float(q[0]) + __uint_as_float(q[1]);
        q = __builtin_amdgcn_permlane32_swap(__float_as_uint(x), __float_as_uint(x), false, false);
        x = __uint_as_float(q[0]) + __uint_as_float(q[1]);
        v[r] = __float_as_uint(x) | 1u;
      }
      if (l < 8) __builtin_amdgcn_raw_buffer_store_b128(v, wr, O_P2 + (unsigned)((s + 1) * 4096 + h * 128 + 32 * w + l * 4) * 4, 0, 16);
      {
        const int wu = __builtin_amdgcn_readfirstlane(w);
#pragma unroll
        for (int i = 0; i < 2; ++i) {
          const int r4 = wu * 8 + i * 4;
          const int rr = r4 + (l >> 4), row = 8 + rr;
          const int cc = (l & 15) ^ (((row & 3) << 2) | ((row >> 2) & 3));
          const char* src = (const char*)p.qkv + ((size_t)((s + 1) * SEG + rr) * NQKV + 4096 + h * 128 + cc * 8) * 2;
          __builtin_amdgcn_global_load_lds((const void*)src, (LAS void*)(smem + L_K + (8 + r4) * 256), 16, 0, 16);
          __builtin_amdgcn_global_load_lds((const void*)(src + 8192), (LAS void*)(smem + L_V + (8 + r4) * 256), 16, 0, 16);
        }
      }
    } else {
      int r = tid >> 5, c = tid & 31;
      u32x4 v = *(const u32x4*)(smem + (c < 16 ? L_K : L_V) + swz(r, c & 15));
      __builtin_amdgcn_raw_buffer_store_b128(v, wr, O_KVM + (unsigned)((s * MM + r) * 8192 + (c >> 4) * 4096 + h * 128 + (c & 15) * 8) * 2, 0, 16);
      asm volatile("s_waitcnt vmcnt(0)" ::: "memory");
      __syncthreads();
      if (threadIdx.x == 0) __hip_atomic_fetch_add(fl + F_M + s * 16, 1u, RLX_AGENT);
    }
  }
}


__device__ __forceinline__ void task_attn(const Params& p, char* smem, int s, int hg) {
  const int tid = threadIdx.x, w = tid >> 6, l = tid & 63, g = l >> 4, c16 = l & 15;
  const int q4 = c16 >> 2, p4 = c16 & 3;
  typedef __attribute__((ext_vector_type(8))) short s16x8;
  const u16* __restrict__ qkv = p.qkv;
  const u16* __restrict__ kvm = p.kvm;
  const float scale = 0.08838834764831845f;
  char* sv = smem + w * 48 * 256;
  const int h = hg * 4 + w;
  u32x4 vst[12];
#pragma unroll
  for (int i = 0; i < 12; ++i) {
    int ch = l + i * 64, r = ch >> 4, c = ch & 15;
    u32x4 v = (u32x4){0, 0, 0, 0};
    if (r < 8) v = *(const u32x4*)(kvm + (size_t)(s * MM + r) * 8192 + 4096 + h * 128 + c * 8);
    else if (r < 40) v = *(const u32x4*)(qkv + (size_t)(s * SEG + r - 8) * NQKV + 8192 + h * 128 + c * 8);
    vst[i] = v;
  }
  bf16x8 qf[4][2], kf[4][3];
#pragma unroll
  for (int t = 0; t < 4; ++t) {
#pragma unroll
    for (int qt = 0; qt < 2; ++qt)
      qf[t][qt] = *(const bf16x8*)(qkv + (size_t)(s * SEG + 16 * qt + c16) * NQKV + h * 128 + 32 * t + 8 * g);
#pragma unroll
    for (int kt = 0; kt < 3; ++kt) {
      int key = 16 * kt + c16;
      const u16* src = key < 8 ? kvm + (size_t)(s * MM + key) * 8192 + h * 128
                               : qkv + (size_t)(s * SEG + (key < 40 ? key - 8 : 0)) * NQKV + 4096 + h * 128;
      kf[t][kt] = *(const bf16x8*)(src + 32 * t + 8 * g);
    }
  }
#pragma unroll
  for (int i = 0; i < 12; ++i) {
    int ch = l + i * 64, r = ch >> 4, c = ch & 15;
    *(u32x4*)(sv + swz(r, c)) = vst[i];
  }
  f32x4 sc[3][2];
#pragma unroll
  for (int kt = 0; kt < 3; ++kt)
#pragma unroll
    for (int qt = 0; qt < 2; ++qt) sc[kt][qt] = (f32x4){0, 0, 0, 0};
#pragma unroll
  for (int t = 0; t < 4; ++t)
#pragma unroll
    for (int kt = 0; kt < 3; ++kt)
#pragma unroll
      for (int qt = 0; qt < 2; ++qt) sc[kt][qt] = __builtin_amdgcn_mfma_f32_16x16x32_bf16(kf[t][kt], qf[t][qt], sc[kt][qt], 0, 0, 0);
  bf16x8 pf0[2], pf1[2];
#pragma unroll
  for (int qt = 0; qt < 2; ++qt) {
    int qi = 8 + 16 * qt + c16;
    float mx = -3.0e38f;
#pragma unroll
    for (int kt = 0; kt < 3; ++kt)
#pragma unroll
      for (int r = 0; r < 4; ++r) {
        int key = 16 * kt + 4 * g + r;
        float v = rbf(rbf(sc[kt][qt][r]) * scale);
        if (key > qi) v = -1e9f;
        if (key >= 40) v = -3.0e38f;
        sc[kt][qt][r] = v;
        mx = fmaxf(mx, v);
      }
    mx = fmaxf(mx, __shfl_xor(mx, 16));
    mx = fmaxf(mx, __shfl_xor(mx, 32));
    float sum = 0.f;
#pragma unroll
    for (int kt = 0; kt < 3; ++kt)
#pragma unroll
      for (int r = 0; r < 4; ++r) {
        int key = 16 * kt + 4 * g + r;
        float e = (key >= 40) ? 0.f : __expf(sc[kt][qt][r] - mx);
        sc[kt][qt][r] = e;
        sum += e;
      }
    sum += __shfl_xor(sum, 16);
    sum += __shfl_xor(sum, 32);
    float inv = 1.f / sum;
    u32x4 b0, b1;
    b0[0] = pack2(sc[0][qt][0] * inv, sc[0][qt][1] * inv); b0[1] = pack2(sc[0][qt][2] * inv, sc[0][qt][3] * inv);
    b0[2] = pack2(sc[1][qt][0] * inv, sc[1][qt][1] * inv); b0[3] = pack2(sc[1][qt][2] * inv, sc[1][qt][3] * inv);
    b1[0] = pack2(sc[2][qt][0] * inv, sc[2][qt][1] * inv); b1[1] = pack2(sc[2][qt][2] * inv, sc[2][qt][3] * inv);
    b1[2] = 0; b1[3] = 0;
    pf0[qt] = __builtin_bit_cast(bf16x8, b0);
    pf1[qt] = __builtin_bit_cast(bf16x8, b1);
  }
  __builtin_amdgcn_s_waitcnt(0xc07f);
  __builtin_amdgcn_wave_barrier();
#pragma unroll
  for (int c = 0; c < 8; ++c) {
    int ch = 2 * c + (p4 >> 1);
    s16x4 a0 = __builtin_amdgcn_ds_read_tr16_b64_v4i16((LAS s16x4*)(sv + swz(4 * g + q4, ch) + 8 * (p4 & 1)));
    s16x4 a1 = __builtin_amdgcn_ds_read_tr16_b64_v4i16((LAS s16x4*)(sv + swz(16 + 4 * g + q4, ch) + 8 * (p4 & 1)));
    s16x4 a2 = __builtin_amdgcn_ds_read_tr16_b64_v4i16((LAS s16x4*)(sv + swz(32 + 4 * g + q4, ch) + 8 * (p4 & 1)));
    s16x8 v0 = __builtin_shufflevector(a0, a1, 0, 1, 2, 3, 4, 5, 6, 7);
    s16x8 v1 = __builtin_shufflevector(a2, a2, 0, 1, 2, 3, 4, 5, 6, 7);
#pragma unroll
    for (int qt = 0; qt < 2; ++qt) {
      f32x4 o = (f32x4){0, 0, 0, 0};
      o = __builtin_amdgcn_mfma_f32_16x16x32_bf16(__builtin_bit_cast(bf16x8, v0), pf0[qt], o, 0, 0, 0);
      o = __builtin_amdgcn_mfma_f32_16x16x32_bf16(__builtin_bit_cast(bf16x8, v1), pf1[qt], o, 0, 0, 0);
      u32x2 v; v[0] = pack2(o[0], o[1]); v[1] = pack2(o[2], o[3]);
      st8_sc1(p.ox + (size_t)(s * SEG + 16 * qt + c16) * 4096 + h * 128 + 16 * c + 4 * g, v);
    }
  }
}

static constexpr int E_WO = 0;
static constexpr int E_RED = 131072;
__device__ __forceinline__ void load_wo_slice(const Params& p, char* smem, int cb) {
  const int tid = threadIdx.x, w = tid >> 6, l = tid & 63, g = l >> 4, c16 = l & 15;
  const int q4 = c16 >> 2, p4 = c16 & 3;
  typedef __attribute__((ext_vector_type(8))) short s16x8;
  char* st = smem + E_RED;
  for (int r8 = 0; r8 < 8; ++r8) {
    u32x4 v[4];
#pragma unroll
    for (int i = 0; i < 4; ++i) {
      int ch = tid + i * 256, r = ch >> 1, c = ch & 1;
      v[i] = *(const u32x4*)(p.Wo + (size_t)(r8 * 512 + r) * DM + cb * 16 + c * 8);
    }
#pragma unroll
    for (int i = 0; i < 4; ++i) {
      int ch = tid + i * 256, r = ch >> 1, c = ch & 1;
      *(u32x4*)(st + r * 32 + c * 16) = v[i];
    }
    __syncthreads();
#pragma unroll
    for (int t = 0; t < 4; ++t) {
      int ks = 4 * w + t;
      int r0 = 32 * ks + 8 * g + q4;
      s16x4 lo = __builtin_amdgcn_ds_read_tr16_b64_v4i16((LAS s16x4*)(st + r0 * 32 + p4 * 8));
      s16x4 hi = __builtin_amdgcn_ds_read_tr16_b64_v4i16((LAS s16x4*)(st + (r0 + 4) * 32 + p4 * 8));
      s16x8 tt = __builtin_shufflevector(lo, hi, 0, 1, 2, 3, 4, 5, 6, 7);
      *(s16x8*)(smem + E_WO + (r8 * 16 + ks) * 1024 + l * 16) = tt;
    }
    __syncthreads();
  }
}
__device__ __forceinline__ void task_out(const Params& p, char* smem, int s, int cb) {
  const int tid = threadIdx.x, w = tid >> 6, l = tid & 63, g = l >> 4, c16 = l & 15;
  f32x4 acc[2];
  acc[0] = (f32x4){0, 0, 0, 0}; acc[1] = (f32x4){0, 0, 0, 0};
  const u16* a0 = p.ox + (size_t)(s * SEG + c16) * 4096 + w * 1024 + 8 * g;
  const u16* a1 = a0 + 16 * 4096;
  const char* wb = smem + E_WO + w * 32768 + l * 16;
#pragma unroll
  for (int hb = 0; hb < 2; ++hb) {
    bf16x8 x0[16], x1[16];
#pragma unroll
    for (int t = 0; t < 16; ++t) { x0[t] = *(const bf16x8*)(a0 + 32 * (hb * 16 + t)); x1[t] = *(const bf16x8*)(a1 + 32 * (hb * 16 + t)); }
    __builtin_amdgcn_sched_barrier(0);
#pragma unroll
    for (int t = 0; t < 16; ++t) {
      bf16x8 wf = *(const bf16x8*)(wb + (hb * 16 + t) * 1024);
      acc[0] = __builtin_amdgcn_mfma_f32_16x16x32_bf16(wf, x0[t], acc[0], 0, 0, 0);
      acc[1] = __builtin_amdgcn_mfma_f32_16x16x32_bf16(wf, x1[t], acc[1], 0, 0, 0);
    }
    __builtin_amdgcn_sched_barrier(0);
  }
  *(f32x4*)(smem + E_RED + (w * 2 + 0) * 1024 + l * 16) = acc[0];
  *(f32x4*)(smem + E_RED + (w * 2 + 1) * 1024 + l * 16) = acc[1];
  __syncthreads();
  if (w < 2) {
    f32x4 a = *(f32x4*)(smem + E_RED + w * 1024 + l * 16);
#pragma unroll
    for (int i = 1; i < 4; ++i) a += *(f32x4*)(smem + E_RED + (i * 2 + w) * 1024 + l * 16);
    int m = s * SEG + 16 * w + c16, n = cb * 16 + 4 * g;
    u32x2 gg = *(const u32x2*)(p.g + (size_t)m * DM + n), xx = *(const u32x2*)(p.x + (size_t)m * DM + n);
    float gf[4] = {bf2f(gg[0] & 0xffff), bf2f(gg[0] >> 16), bf2f(gg[1] & 0xffff), bf2f(gg[1] >> 16)};
    float xf[4] = {bf2f(xx[0] & 0xffff), bf2f(xx[0] >> 16), bf2f(xx[1] & 0xffff), bf2f(xx[1] >> 16)};
    float r[4];
#pragma unroll
    for (int i = 0; i < 4; ++i) r[i] = xf[i] + rbf(rbf(a[i]) * gf[i]);
    u32x2 o; o[0] = pack2(r[0], r[1]); o[1] = pack2(r[2], r[3]);
    *(u32x2*)(p.out + (size_t)m * DM + n) = o;
  }
  __syncthreads();
}

__device__ __forceinline__ void worker(const Params& p, char* smem) {
  constexpr int NWK = 224;
  const int wid = blockIdx.x - NH;
  for (int t = wid; t < 1024; t += NWK) {
    int rb = t >> 6, ct = t & 63;
    const u16* B = ct < 32 ? p.Wk : p.Wv;
    EpiStore epi{p.qkv + 4096 + (ct >> 5) * 4096, NQKV, 0};
    gemm_tile<128, 128, 64, EpiStore>(smem, p.xn, DM, B, 4096, S_, 4096, DM, rb * 128, (ct & 31) * 128, epi);
    publish(p.fl + F_TILE + t, p.fl + F_QKV + rb * 16);
  }
  for (int t = (wid + 224 - (1024 % NWK)) % NWK; t < 176; t += NWK) {
    int rb = t / 11, ct = t % 11;
    EpiGelu epi{p.h1, FF, 0, p.b1};
    gemm_tile<128, 128, 64, EpiGelu>(smem, p.y, DM, p.w1, FF, S_, FF, DM, rb * 128, ct * 128, epi);
    publish(p.fl + F_H + rb * 16, nullptr);
  }
  for (int t = (wid + 48) % NWK; t < 512; t += NWK) {
    int rb = t >> 5, ct = t & 31;
    EpiStore epi{p.qkv, NQKV, 0};
    gemm_tile<128, 128, 64, EpiStore>(smem, p.xn, DM, p.Wq, 4096, S_, 4096, DM, rb * 128, ct * 128, epi);
    publish(p.fl + F_QKV + rb * 16, nullptr);
  }
  for (int t = (wid + 120) % NWK; t < 64; t += NWK) {
    int rb = t >> 2, ct = t & 3;
    if (threadIdx.x == 0) wait1<64>(p.fl + F_H + rb * 16, 11u);
    acquire_all();
    EpiSilu epi{p.g, DM, 0, p.b2};
    gemm_tile<128, 128, 64, EpiSilu>(smem, p.h1, FF, p.w2, DM, S_, DM, FF, rb * 128, ct * 128, epi);
    publish(p.fl + F_G + rb * 16, nullptr);
  }
  if (wid < 32) {
    for (int s = wid >> 3; s < NSEG; s += 4) {
      if (threadIdx.x == 0) {
        wait1<64>(p.fl + F_QKV + (s >> 2) * 16, 96u);
        if (s >= 60) wait1<8>(p.fl + F_M + s * 16, 32u); else wait1<64>(p.fl + F_M + s * 16, 32u);
      }
      acquire_all();
      task_attn(p, smem, s, wid & 7);
      asm volatile("s_waitcnt vmcnt(0)" ::: "memory");
      __syncthreads();
      if (threadIdx.x == 0) {
        unsigned old = __hip_atomic_fetch_add(p.fl + F_OX + s * 16, 1u, RLX_AGENT);
        if (old == 7u)
          for (int c = 0; c < 32; ++c) __hip_atomic_store(p.fl + F_OXB + (s * 32 + c) * 16, 1u, RLX_AGENT);
      }
    }
  } else {
    const int j = wid - 32, cb = j & 31, grp = j >> 5;
    load_wo_slice(p, smem, cb);
    for (int s = grp; s < NSEG; s += 6) {
      if (threadIdx.x == 0) {
        wait1<64>(p.fl + F_G + (s >> 2) * 16, 4u);
        if (s >= 60) wait1<8>(p.fl + F_OXB + (s * 32 + cb) * 16, 1u); else wait1<64>(p.fl + F_OXB + (s * 32 + cb) * 16, 1u);
      }
      acquire_all();
      task_out(p, smem, s, cb);
    }
  }
}

__global__ __launch_bounds__(256, 1) void k_main(Params p) {
  extern __shared__ __attribute__((aligned(16))) char smem[];
  if (blockIdx.x < NH) recurrence(p, smem);
  else worker(p, smem);
}

extern "C" void kernel_launch(void* const* d_in, const int* in_sizes, int n_in, void* d_out, int out_size, void* d_ws,
                              size_t ws_size, hipStream_t stream) {
  Params p;
  p.x = (const u16*)d_in[0];
  p.mem0 = (const u16*)d_in[1];
  p.Wq = (const u16*)d_in[2];
  p.Wk = (const u16*)d_in[3];
  p.Wv = (const u16*)d_in[4];
  p.Wo = (const u16*)d_in[5];
  p.w1 = (const u16*)d_in[8];
  p.b1 = (const u16*)d_in[9];
  p.w2 = (const u16*)d_in[10];
  p.b2 = (const u16*)d_in[11];
  p.out = (u16*)d_out;
  char* ws = (char*)d_ws;
  size_t o = 0;
  auto take = [&](size_t bytes) { char* q = ws + o; o += (bytes + 255) & ~(size_t)255; return q; };
  p.fl = (unsigned*)take(F_WORDS * 4);
  p.P2 = (float*)take((size_t)NSEG * 4096 * 4);
  p.P1 = (float*)take((size_t)NSEG * 32 * 32 * 128 * 4);
  const size_t zbytes = o;
  p.xn = (u16*)take((size_t)S_ * DM * 2);
  p.y = (u16*)take((size_t)S_ * DM * 2);
  p.qkv = (u16*)take((size_t)S_ * NQKV * 2);
  p.h1 = (u16*)take((size_t)S_ * FF * 2);
  p.g = (u16*)take((size_t)S_ * DM * 2);
  p.kvm = (u16*)take((size_t)NSEG * MM * 8192 * 2);
  p.ox = (u16*)take((size_t)S_ * 4096 * 2);

  k_prep<<<512, 256, 0, stream>>>(p.x, p.xn, p.y, (float*)ws, (int)(zbytes / 16));
  hipFuncSetAttribute((const void*)k_main, hipFuncAttributeMaxDynamicSharedMemorySize, 163840);
  k_main<<<256, 256, 163840, stream>>>(p);
}
```
